```python
import torch

def _softmax(x, axis=-1):
    return torch.softmax(x, dim=axis)
def _gelu(x):
    return torch.nn.functional.gelu(x, approximate="tanh")
def _silu(x):
    return torch.nn.functional.silu(x)
def _sigmoid(x):
    return torch.sigmoid(x)
def _layernorm(x, axis=-1, eps=1e-5):
    m = x.mean(dim=axis, keepdim=True)
    v = x.var(dim=axis, keepdim=True, unbiased=False)
    return (x - m) / torch.sqrt(v + eps)
def _scatter_add(tgt, idx, src, axis=0):
    return tgt.clone().index_add_(axis, idx, src)
def _segment_sum(src, seg, n):
    out = torch.zeros((n,) + src.shape[1:], dtype=src.dtype)
    return out.index_add_(0, seg, src)
def _pad(x, pw):
    flat = tuple(v for pair in reversed(pw) for v in pair)
    return torch.nn.functional.pad(x, flat)

kernel_name = 'concat_heads__mha_block_cached__mha_block_cached__decode'

def setup_inputs():

    _w = (lambda a: (lambda t: t.to(torch.bfloat16) if t.is_floating_point() else t)(torch.as_tensor(a)))
    def _A_setup():
        B, Sq, D = 2, 1, 448
        H, Sk, Dh = 16, 4096, 256
        return {
            "x": _w((lambda *s: torch.randn(*s, dtype=torch.bfloat16) * 0.8)(B, Sq, D)),
            "k_cache": _w((lambda *s: torch.randn(*s, dtype=torch.bfloat16) * 0.8)(B, H, Sk, Dh)),
            "v_cache": _w((lambda *s: torch.randn(*s, dtype=torch.bfloat16) * 0.05)(B, H, Sk, Dh)),
            "wq": _w((lambda *s: torch.randn(*s, dtype=torch.bfloat16) * 0.8)(D, H * Dh) * (D ** -0.5)),
            "wo": _w((lambda *s: torch.randn(*s, dtype=torch.bfloat16) * 0.05)(H * Dh, D) * ((H * Dh) ** -0.5)),
        }
    _A_in = _A_setup()

    def _B_setup():
        B, Sq, D = 2, 1, 448
        H, Sk, Dh = 16, 4096, 256
        return {
            "x": _w((lambda *s: torch.randn(*s, dtype=torch.bfloat16) * 0.8)(B, Sq, D)),
            "k_cache": _w((lambda *s: torch.randn(*s, dtype=torch.bfloat16) * 0.8)(B, H, Sk, Dh)),
            "v_cache": _w((lambda *s: torch.randn(*s, dtype=torch.bfloat16) * 0.05)(B, H, Sk, Dh)),
            "wq": _w((lambda *s: torch.randn(*s, dtype=torch.bfloat16) * 0.8)(D, H * Dh) * (D ** -0.5)),
            "wo": _w((lambda *s: torch.randn(*s, dtype=torch.bfloat16) * 0.05)(H * Dh, D) * ((H * Dh) ** -0.5)),
        }
    _B_in = _B_setup()

    return {
        "A_x": _A_in["x"],
        "A_k_cache": _A_in["k_cache"],
        "A_v_cache": _A_in["v_cache"],
        "A_wq": _A_in["wq"],
        "A_wo": _A_in["wo"],
        "B_x": _B_in["x"],
        "B_k_cache": _B_in["k_cache"],
        "B_v_cache": _B_in["v_cache"],
        "B_wq": _B_in["wq"],
        "B_wo": _B_in["wo"],
    }

def reference(A_x, A_k_cache, A_v_cache, A_wq, A_wo, B_x, B_k_cache, B_v_cache, B_wq, B_wo):
    def _A_ref(x, k_cache, v_cache, wq, wo):
        B, Sq, D = x.shape
        H, Dh = 16, 256
        q = (lambda x, *d: x.permute(*d))((x @ wq).reshape(B, Sq, H, Dh), 0, 2, 1, 3)
        scores = torch.einsum("bhqd,bhkd->bhqk", q, k_cache) * (Dh ** -0.5)
        p = _softmax(scores, axis=-1)
        o = torch.einsum("bhqk,bhkd->bhqd", p, v_cache)
        return (lambda x, *d: x.permute(*d))(o, 0, 2, 1, 3).reshape(B, Sq, H * Dh) @ wo

    def _B_ref(x, k_cache, v_cache, wq, wo):
        B, Sq, D = x.shape
        H, Dh = 16, 256
        q = (lambda x, *d: x.permute(*d))((x @ wq).reshape(B, Sq, H, Dh), 0, 2, 1, 3)
        scores = torch.einsum("bhqd,bhkd->bhqk", q, k_cache) * (Dh ** -0.5)
        p = _softmax(scores, axis=-1)
        o = torch.einsum("bhqk,bhkd->bhqd", p, v_cache)
        return (lambda x, *d: x.permute(*d))(o, 0, 2, 1, 3).reshape(B, Sq, H * Dh) @ wo

    a = _A_ref(A_x, A_k_cache, A_v_cache, A_wq, A_wo)
    b = _B_ref(B_x, B_k_cache, B_v_cache, B_wq, B_wo)
    return torch.concatenate([a, b], axis=-1)
```

```cpp
#include <hip/hip_runtime.h>
#include <hip/hip_bf16.h>
#include <stdint.h>

#define GAS __attribute__((address_space(1)))
typedef unsigned v4u __attribute__((ext_vector_type(4)));
typedef unsigned v2u __attribute__((ext_vector_type(2)));
typedef float f32x4 __attribute__((ext_vector_type(4)));
typedef __bf16 bf16x2 __attribute__((ext_vector_type(2)));
typedef GAS unsigned gu32;
typedef GAS unsigned long long gu64;
#define RLX_AGENT __ATOMIC_RELAXED, __HIP_MEMORY_SCOPE_AGENT

#define WIN 16
#define PRE 4

constexpr int H = 16, SK = 4096, DH = 256, DM = 448;
constexpr int NWAVE = 8, NTHR = 512;
constexpr int NLD = 64;
constexpr size_t WS_EPOCH = 0;
constexpr size_t WS_INBOX = 4096;
constexpr unsigned SPIN_CAP = 1u << 20;

struct Params {
  const void* x[2];
  const void* k[2];
  const void* v[2];
  const void* wq[2];
  const void* wo[2];
  void* out;
  void* ws;
};

__device__ __forceinline__ float dot2(unsigned a, unsigned b, float c) {
  return __builtin_amdgcn_fdot2_f32_bf16(__builtin_bit_cast(bf16x2, a), __builtin_bit_cast(bf16x2, b), c, false);
}
__device__ __forceinline__ unsigned pack_bf16(float lo, float hi) {
  bf16x2 r;
  r.x = (__bf16)lo;
  r.y = (__bf16)hi;
  return __builtin_bit_cast(unsigned, r);
}
template <int CTRL> __device__ __forceinline__ float dppf(float x) {
  return __builtin_bit_cast(float, __builtin_amdgcn_mov_dpp(__builtin_bit_cast(int, x), CTRL, 0xf, 0xf, true));
}
template <int CTRL> __device__ __forceinline__ unsigned dppu(unsigned x) {
  return (unsigned)__builtin_amdgcn_mov_dpp((int)x, CTRL, 0xf, 0xf, true);
}
constexpr int XOR1 = 0xB1, XOR2 = 0x4E, XOR7 = 0x141, XOR8 = 0x128, ROR4 = 0x124;
__device__ __forceinline__ float swap16_add(float a, float b) {
  auto r = __builtin_amdgcn_permlane16_swap(__float_as_uint(a), __float_as_uint(b), false, false);
  return __uint_as_float(r[0]) + __uint_as_float(r[1]);
}
__device__ __forceinline__ float swap32_add(float a, float b) {
  auto r = __builtin_amdgcn_permlane32_swap(__float_as_uint(a), __float_as_uint(b), false, false);
  return __uint_as_float(r[0]) + __uint_as_float(r[1]);
}
__device__ __forceinline__ float swap16_max(float a) {
  auto r = __builtin_amdgcn_permlane16_swap(__float_as_uint(a), __float_as_uint(a), false, false);
  return fmaxf(__uint_as_float(r[0]), __uint_as_float(r[1]));
}
__device__ __forceinline__ float swap32_max(float a) {
  auto r = __builtin_amdgcn_permlane32_swap(__float_as_uint(a), __float_as_uint(a), false, false);
  return fmaxf(__uint_as_float(r[0]), __uint_as_float(r[1]));
}
__device__ __forceinline__ float wave_max(float v) {
  v = fmaxf(v, dppf<XOR1>(v));
  v = fmaxf(v, dppf<XOR2>(v));
  v = fmaxf(v, dppf<XOR7>(v));
  v = fmaxf(v, dppf<XOR8>(v));
  v = swap16_max(v);
  return swap32_max(v);
}
__device__ __forceinline__ float wave_sum(float v) {
  v += dppf<XOR1>(v);
  v += dppf<XOR2>(v);
  v += dppf<XOR7>(v);
  v += dppf<XOR8>(v);
  v = swap16_add(v, v);
  return swap32_add(v, v);
}
typedef __amdgpu_buffer_rsrc_t rsrc_t;
__device__ __forceinline__ rsrc_t mk_rsrc(const void* base, unsigned bytes) {
  return __builtin_amdgcn_make_buffer_rsrc((void*)base, 0, bytes, 0x00020000);
}
constexpr int AUX_NT = 2, AUX_SC1 = 16;
template <int AUX> __device__ __forceinline__ v4u bld(rsrc_t rs, unsigned voff, unsigned soff) {
  return __builtin_amdgcn_raw_buffer_load_b128(rs, voff, soff, AUX);
}
#define SB() __builtin_amdgcn_sched_barrier(0)
#define LDS_BARRIER()                                      \
  do {                                                     \
    asm volatile("s_waitcnt lgkmcnt(0)" ::: "memory");     \
    __builtin_amdgcn_s_barrier();                          \
    asm volatile("" ::: "memory");                         \
  } while (0)

constexpr int L_QP = 0;
constexpr int L_QB = L_QP + 16 * 256;
constexpr int L_P = L_QB + 128;
constexpr int L_ACC = L_P + 8 * 128;
constexpr int L_ML = L_ACC + 8 * 256;
constexpr int L_TAG = L_ML + 16;
constexpr int L_END = L_TAG + 4;

__global__ void __launch_bounds__(NTHR, 2) mha2_decode_kernel(Params prm) {
  __shared__ __attribute__((aligned(16))) float lds[L_END];
  const int tid = threadIdx.x, lane = tid & 63;
  const int w = __builtin_amdgcn_readfirstlane(tid >> 6);
  const int bid = blockIdx.x;
  const int slot = bid >> 3;
  const int pr = (bid & 7) * 4 + (slot >> 3);
  const int blk = pr >> 4, h = pr & 15;
  const int b = (slot >> 2) & 1, s = slot & 3;
  const int hh = lane >> 5, l32 = lane & 31;
  const int grp = blk * 2 + b, me = s * 16 + h;

  const size_t kvoff = ((size_t)(b * H + h) * SK + s * 1024 + w * 128) * DH * 2;
  const rsrc_t rk = mk_rsrc((const char*)prm.k[blk] + kvoff, NLD * 1024);
  const rsrc_t rv = mk_rsrc((const char*)prm.v[blk] + kvoff, NLD * 1024);
  const unsigned l16 = lane * 16;
  GAS char* ws = (GAS char*)prm.ws;

  v4u ring[WIN];
  v4u wb[32];
#pragma unroll
  for (int i = 0; i < PRE; ++i) ring[i] = bld<AUX_NT>(rk, l16, i * 1024);
  SB();
  const unsigned epoch = __hip_atomic_load((gu32*)(ws + WS_EPOCH + grp * 256), RLX_AGENT);
  v2u xp[7];
  {
    const GAS v2u* xq = (const GAS v2u*)((const GAS char*)prm.x[blk] + b * (DM * 2) + w * 112 + hh * 56);
#pragma unroll
    for (int i = 0; i < 7; ++i) xp[i] = xq[i];
    SB();
    const rsrc_t rq = mk_rsrc((const char*)prm.wq[blk] + (size_t)(w * 56) * (H * DH * 2) + h * (DH * 2), 55 * (H * DH * 2) + DH * 2);
    const unsigned qoff = hh * 28 * (H * DH * 2) + l32 * 16;
#pragma unroll
    for (int i = 0; i < 28; ++i) wb[i] = bld<0>(rq, qoff, i * (H * DH * 2));
  }
  SB();
#pragma unroll
  for (int i = PRE; i < WIN; ++i) ring[i] = bld<AUX_NT>(rk, l16, i * 1024);
  SB();
  {
    float qa[8];
#pragma unroll
    for (int j = 0; j < 8; ++j) qa[j] = 0.f;
#pragma unroll
    for (int m = 0; m < 14; ++m) {
      const unsigned xv = xp[m >> 1][m & 1];
#pragma unroll
      for (int j = 0; j < 4; ++j) {
        const unsigned a = wb[2 * m][j], c = wb[2 * m + 1][j];
        qa[2 * j] = dot2(__builtin_amdgcn_perm(c, a, 0x05040100u), xv, qa[2 * j]);
        qa[2 * j + 1] = dot2(__builtin_amdgcn_perm(c, a, 0x07060302u), xv, qa[2 * j + 1]);
      }
    }
    f32x4* dst = (f32x4*)(lds + L_QP + (w * 2 + hh) * 256 + l32 * 8);
    dst[0] = f32x4{qa[0], qa[1], qa[2], qa[3]};
    dst[1] = f32x4{qa[4], qa[5], qa[6], qa[7]};
    if (tid == 0) ((unsigned*)lds)[L_TAG] = epoch + 1u;
  }
  LDS_BARRIER();
  if (tid < 128) {
    float q0 = 0.f, q1 = 0.f;
#pragma unroll
    for (int i = 0; i < 16; ++i) {
      const float2 t = *(const float2*)(lds + L_QP + i * 256 + tid * 2);
      q0 += t.x;
      q1 += t.y;
    }
    ((unsigned*)lds)[L_QB + tid] = pack_bf16(q0, q1);
  }
  LDS_BARRIER();
  const v4u qv = *(const v4u*)(lds + L_QB + l32 * 4);

  const int rc = lane >> 3, c8 = lane & 7, qd = (lane >> 2) & 1;
  const rsrc_t ro = mk_rsrc((const char*)prm.wo[blk] + (size_t)h * DH * (DM * 2) + w * 112, 255 * (DM * 2) + 112);
  const int cc = c8 < 7 ? c8 : 6;
  const unsigned woA = (unsigned)((32 * rc + 16 * qd) * (DM * 2) + cc * 16);
  const unsigned woB = (unsigned)((32 * rc + 16 * (1 - qd)) * (DM * 2) + cc * 16);

  float sc[2];
  {
    float xs[16];
    float half_red[2];
#pragma unroll
    for (int t = 0; t < NLD; ++t) {
      const v4u cur = ring[t % WIN];
      float d = dot2(cur[0], qv[0], 0.f);
      d = dot2(cur[1], qv[1], d);
      d = dot2(cur[2], qv[2], d);
      d = dot2(cur[3], qv[3], d);
      xs[t & 15] = d;
      SB();
      {
        const int n = t + WIN;
        ring[t % WIN] = n < NLD ? bld<AUX_NT>(rk, l16, n * 1024) : bld<AUX_NT>(rv, l16, (n - NLD) * 1024);
      }
      if (t < 32) wb[t] = bld<0>(ro, t < 16 ? woA : woB, (t & 15) * (DM * 2));
      SB();
      if ((t & 15) == 15) {
#define TR_STEP(HALF, CTRL, BIT)                                                   \
  _Pragma("unroll") for (int u = 0; u < (HALF); ++u) {                             \
    const float lo_ = xs[u], hi_ = xs[u + (HALF)];                                 \
    const float t1_ = lo_ + dppf<CTRL>(lo_), t2_ = hi_ + dppf<CTRL>(hi_);          \
    xs[u] = (lane & (BIT)) ? t2_ : t1_;                                            \
  }
        TR_STEP(8, XOR8, 8) TR_STEP(4, XOR7, 4) TR_STEP(2, XOR2, 2) TR_STEP(1, XOR1, 1)
#undef TR_STEP
        half_red[(t >> 4) & 1] = xs[0];
        if ((t & 31) == 31) sc[t >> 5] = swap16_add(half_red[0], half_red[1]);
        SB();
      }
    }
  }
  constexpr float C2 = 0.0625f * 1.4426950408889634f;
  const float mw = wave_max(fmaxf(sc[0], sc[1])) * C2;
  const float p0 = __builtin_amdgcn_exp2f(fmaf(sc[0], C2, -mw));
  const float p1 = __builtin_amdgcn_exp2f(fmaf(sc[1], C2, -mw));
  const float lw = wave_sum(p0 + p1);
  lds[L_P + w * 128 + hh * 64 + l32] = p0;
  lds[L_P + w * 128 + hh * 64 + 32 + l32] = p1;
  if (lane == 0) {
    lds[L_ML + w] = mw;
    lds[L_ML + 8 + w] = lw;
  }
  asm volatile("s_waitcnt lgkmcnt(0)" ::: "memory");
  SB();

  float acc[8];
#pragma unroll
  for (int j = 0; j < 8; ++j) acc[j] = 0.f;
  {
    const f32x4* pl = (const f32x4*)(lds + L_P + w * 128 + hh * 64);
    f32x4 pc = pl[0];
#pragma unroll
    for (int t = 0; t < NLD; ++t) {
      const v4u cur = ring[(t + NLD) % WIN];
      const float pv = pc[t & 3];
      if ((t & 3) == 3 && t + 1 < NLD) pc = pl[(t + 1) >> 2];
#pragma unroll
      for (int j = 0; j < 4; ++j) {
        acc[2 * j] = fmaf(pv, __uint_as_float(cur[j] << 16), acc[2 * j]);
        acc[2 * j + 1] = fmaf(pv, __uint_as_float(cur[j] & 0xffff0000u), acc[2 * j + 1]);
      }
      SB();
      if (t + WIN < NLD) ring[(t + NLD) % WIN] = bld<AUX_NT>(rv, l16, (t + WIN) * 1024);
      SB();
      if (t < 16) {
#pragma unroll
        for (int j = 0; j < 4; ++j) {
          const unsigned a = wb[2 * t][j], c = wb[2 * t + 1][j];
          wb[2 * t][j] = __builtin_amdgcn_perm(c, a, 0x05040100u);
          wb[2 * t + 1][j] = __builtin_amdgcn_perm(c, a, 0x07060302u);
        }
        SB();
      }
    }
  }
  {
    f32x4 r;
#pragma unroll
    for (int j = 0; j < 4; ++j) r[j] = swap32_add(acc[j], acc[j + 4]);
    *(f32x4*)(lds + L_ACC + w * 256 + l32 * 8 + hh * 4) = r;
  }
  LDS_BARRIER();
  float lse;
  unsigned o0, o1;
  {
    const f32x4 ma = *(const f32x4*)(lds + L_ML), mb = *(const f32x4*)(lds + L_ML + 4);
    const f32x4 la = *(const f32x4*)(lds + L_ML + 8), lb = *(const f32x4*)(lds + L_ML + 12);
    f32x4 a[8];
#pragma unroll
    for (int i = 0; i < 8; ++i) a[i] = *(const f32x4*)(lds + L_ACC + i * 256 + lane * 4);
    const float M = fmaxf(fmaxf(fmaxf(ma[0], ma[1]), fmaxf(ma[2], ma[3])), fmaxf(fmaxf(mb[0], mb[1]), fmaxf(mb[2], mb[3])));
    float e[8];
#pragma unroll
    for (int i = 0; i < 4; ++i) {
      e[i] = __builtin_amdgcn_exp2f(ma[i] - M);
      e[i + 4] = __builtin_amdgcn_exp2f(mb[i] - M);
    }
    float L = 0.f;
#pragma unroll
    for (int i = 0; i < 4; ++i) L = fmaf(e[i], la[i], L);
#pragma unroll
    for (int i = 0; i < 4; ++i) L = fmaf(e[i + 4], lb[i], L);
    f32x4 o = a[0] * e[0];
#pragma unroll
    for (int i = 1; i < 8; ++i) o += a[i] * e[i];
    const float inv = 1.0f / L;
    o *= inv;
    lse = M + __builtin_amdgcn_logf(L);
    o0 = pack_bf16(o[0], o[1]);
    o1 = pack_bf16(o[2], o[3]);
  }
  const unsigned tag = ((const unsigned*)lds)[L_TAG];
  float y[8];
#pragma unroll
  for (int j = 0; j < 8; ++j) y[j] = 0.f;
  {
    const unsigned m0 = dppu<XOR7>(o0), m1 = dppu<XOR7>(o1);
#define WO_STEP(OV, I)                                        \
  {                                                           \
    const unsigned ov_ = (OV);                                \
    _Pragma("unroll") for (int j = 0; j < 4; ++j) {           \
      y[2 * j] = dot2(wb[2 * (I)][j], ov_, y[2 * j]);         \
      y[2 * j + 1] = dot2(wb[2 * (I) + 1][j], ov_, y[2 * j + 1]); \
    }                                                         \
  }
    WO_STEP(dppu<0x00>(o0), 0) WO_STEP(dppu<0x00>(o1), 1)
    WO_STEP(dppu<0x55>(o0), 2) WO_STEP(dppu<0x55>(o1), 3)
    WO_STEP(dppu<0xAA>(o0), 4) WO_STEP(dppu<0xAA>(o1), 5)
    WO_STEP(dppu<0xFF>(o0), 6) WO_STEP(dppu<0xFF>(o1), 7)
    WO_STEP(dppu<0xFF>(m0), 8) WO_STEP(dppu<0xFF>(m1), 9)
    WO_STEP(dppu<0xAA>(m0), 10) WO_STEP(dppu<0xAA>(m1), 11)
    WO_STEP(dppu<0x55>(m0), 12) WO_STEP(dppu<0x55>(m1), 13)
    WO_STEP(dppu<0x00>(m0), 14) WO_STEP(dppu<0x00>(m1), 15)
#undef WO_STEP
  }
  float yv;
  {
    float z[4], u[2];
#pragma unroll
    for (int j = 0; j < 4; ++j) z[j] = swap32_add(y[j], y[j + 4]);
#pragma unroll
    for (int j = 0; j < 2; ++j) u[j] = swap16_add(z[j], z[j + 2]);
    const float t0 = u[0] + dppf<XOR8>(u[0]), t1 = u[1] + dppf<XOR8>(u[1]);
    yv = (lane & 8) ? t1 : t0;
  }
  if (c8 == 7) yv = lse;
  {
    gu64* g = (gu64*)(ws + WS_INBOX + ((size_t)((grp * 64 + w * 8 + rc) * 64 + me) * 64 + c8 * 8));
    __hip_atomic_store(g, ((unsigned long long)tag << 32) | __float_as_uint(yv), RLX_AGENT);
  }
  if (w != 0) return;

  {
    const rsrc_t rs = mk_rsrc((const char*)prm.ws + WS_INBOX + (size_t)(grp * 64 + me) * 4096, 4096);
    v4u g[4];
    for (unsigned spins = 0;; ++spins) {
#pragma unroll
      for (int t = 0; t < 4; ++t) g[t] = bld<AUX_SC1>(rs, l16, t * 1024);
      bool ok = true;
#pragma unroll
      for (int t = 0; t < 4; ++t) ok = ok && (g[t][1] == tag) && (g[t][3] == tag);
      if (__all(ok) || spins > SPIN_CAP) break;
      __builtin_amdgcn_s_sleep(2);
    }
    float ls[4];
#pragma unroll
    for (int t = 0; t < 4; ++t) ls[t] = dppf<0xFF>(__uint_as_float(g[t][2]));
    const float M = fmaxf(fmaxf(ls[0], ls[1]), fmaxf(ls[2], ls[3]));
    float e[4], L = 0.f;
#pragma unroll
    for (int t = 0; t < 4; ++t) {
      e[t] = __builtin_amdgcn_exp2f(ls[t] - M);
      L += e[t];
    }
    float r0 = 0.f, r1 = 0.f;
#pragma unroll
    for (int t = 0; t < 4; ++t) {
      r0 = fmaf(e[t], __uint_as_float(g[t][0]), r0);
      r1 = fmaf(e[t], __uint_as_float(g[t][2]), r1);
    }
    const float inv = 1.0f / L;
    r0 *= inv;
    r1 *= inv;
    r0 += dppf<ROR4>(r0);
    r1 += dppf<ROR4>(r1);
    r0 += dppf<XOR8>(r0);
    r1 += dppf<XOR8>(r1);
    float r = swap16_add(r0, r1);
    r = swap32_add(r, r);
    const int cpos = 2 * (lane & 3) + ((lane >> 4) & 1);
    if ((lane & 44) == 0 && cpos < 7) {
      const int col = 56 * (me >> 3) + 8 * cpos + (me & 7);
      const unsigned short ob = (unsigned short)(pack_bf16(r, r) & 0xffffu);
      __hip_atomic_store((GAS unsigned short*)prm.out + b * (2 * DM) + blk * DM + col, ob, RLX_AGENT);
    }
    if (me == 0 && lane == 0) __hip_atomic_store((gu32*)(ws + WS_EPOCH + grp * 256), tag, RLX_AGENT);
  }
}

extern "C" void kernel_launch(void* const* d_in, const int* in_sizes, int n_in, void* d_out, int out_size, void* d_ws,
                              size_t ws_size, hipStream_t stream) {
  Params p;
  p.x[0] = d_in[0];
  p.k[0] = d_in[1];
  p.v[0] = d_in[2];
  p.wq[0] = d_in[3];
  p.wo[0] = d_in[4];
  p.x[1] = d_in[5];
  p.k[1] = d_in[6];
  p.v[1] = d_in[7];
  p.wq[1] = d_in[8];
  p.wo[1] = d_in[9];
  p.out = d_out;
  p.ws = d_ws;
  mha2_decode_kernel<<<dim3(256), dim3(NTHR), 0, stream>>>(p);
}
```

```cpp
#define KV_AUX 2
#define W_AUX 0
#include <hip/hip_runtime.h>
#include <hip/hip_bf16.h>
#include <stdint.h>

#define GAS __attribute__((address_space(1)))
typedef unsigned v4u __attribute__((ext_vector_type(4)));
typedef unsigned v2u __attribute__((ext_vector_type(2)));
typedef float f32x4 __attribute__((ext_vector_type(4)));
typedef __bf16 bf16x2 __attribute__((ext_vector_type(2)));
typedef GAS unsigned gu32;
typedef GAS unsigned long long gu64;
#define RLX_AGENT __ATOMIC_RELAXED, __HIP_MEMORY_SCOPE_AGENT

#define WIN 16
#define PRE 4

constexpr int H = 16, SK = 4096, DH = 256, DM = 448;
constexpr int NWAVE = 8, NTHR = 512;
constexpr int NLD = 64;
constexpr size_t WS_EPOCH = 0;
constexpr size_t WS_INBOX = 4096;
constexpr unsigned SPIN_CAP = 1u << 20;

struct Params {
  const void* x[2];
  const void* k[2];
  const void* v[2];
  const void* wq[2];
  const void* wo[2];
  void* out;
  void* ws;
};

__device__ __forceinline__ float dot2(unsigned a, unsigned b, float c) {
  return __builtin_amdgcn_fdot2_f32_bf16(__builtin_bit_cast(bf16x2, a), __builtin_bit_cast(bf16x2, b), c, false);
}
__device__ __forceinline__ unsigned pack_bf16(float lo, float hi) {
  bf16x2 r;
  r.x = (__bf16)lo;
  r.y = (__bf16)hi;
  return __builtin_bit_cast(unsigned, r);
}
template <int CTRL> __device__ __forceinline__ float dppf(float x) {
  return __builtin_bit_cast(float, __builtin_amdgcn_mov_dpp(__builtin_bit_cast(int, x), CTRL, 0xf, 0xf, true));
}
template <int CTRL> __device__ __forceinline__ unsigned dppu(unsigned x) {
  return (unsigned)__builtin_amdgcn_mov_dpp((int)x, CTRL, 0xf, 0xf, true);
}
constexpr int XOR1 = 0xB1, XOR2 = 0x4E, XOR7 = 0x141, XOR8 = 0x128, ROR4 = 0x124;
__device__ __forceinline__ float swap16_add(float a, float b) {
  auto r = __builtin_amdgcn_permlane16_swap(__float_as_uint(a), __float_as_uint(b), false, false);
  return __uint_as_float(r[0]) + __uint_as_float(r[1]);
}
__device__ __forceinline__ float swap32_add(float a, float b) {
  auto r = __builtin_amdgcn_permlane32_swap(__float_as_uint(a), __float_as_uint(b), false, false);
  return __uint_as_float(r[0]) + __uint_as_float(r[1]);
}
__device__ __forceinline__ float swap16_max(float a) {
  auto r = __builtin_amdgcn_permlane16_swap(__float_as_uint(a), __float_as_uint(a), false, false);
  return fmaxf(__uint_as_float(r[0]), __uint_as_float(r[1]));
}
__device__ __forceinline__ float swap32_max(float a) {
  auto r = __builtin_amdgcn_permlane32_swap(__float_as_uint(a), __float_as_uint(a), false, false);
  return fmaxf(__uint_as_float(r[0]), __uint_as_float(r[1]));
}
__device__ __forceinline__ float wave_max(float v) {
  v = fmaxf(v, dppf<XOR1>(v));
  v = fmaxf(v, dppf<XOR2>(v));
  v = fmaxf(v, dppf<XOR7>(v));
  v = fmaxf(v, dppf<XOR8>(v));
  v = swap16_max(v);
  return swap32_max(v);
}
__device__ __forceinline__ float wave_sum(float v) {
  v += dppf<XOR1>(v);
  v += dppf<XOR2>(v);
  v += dppf<XOR7>(v);
  v += dppf<XOR8>(v);
  v = swap16_add(v, v);
  return swap32_add(v, v);
}
typedef __amdgpu_buffer_rsrc_t rsrc_t;
__device__ __forceinline__ rsrc_t mk_rsrc(const void* base, unsigned bytes) {
  return __builtin_amdgcn_make_buffer_rsrc((void*)base, 0, bytes, 0x00020000);
}
constexpr int AUX_NT = KV_AUX, AUX_SC1 = 16;
template <int AUX> __device__ __forceinline__ v4u bld(rsrc_t rs, unsigned voff, unsigned soff) {
  return __builtin_amdgcn_raw_buffer_load_b128(rs, voff, soff, AUX);
}
#define SB() __builtin_amdgcn_sched_barrier(0)
#define LDS_BARRIER()                                      \
  do {                                                     \
    asm volatile("s_waitcnt lgkmcnt(0)" ::: "memory");     \
    __builtin_amdgcn_s_barrier();                          \
    asm volatile("" ::: "memory");                         \
  } while (0)

#define ST(k)
#define ST_FLUSH()
constexpr int L_QP = 0;
constexpr int L_QB = L_QP + 16 * 256;
constexpr int L_P = L_QB + 128;
constexpr int L_ACC = L_P + 8 * 128;
constexpr int L_ML = L_ACC + 8 * 256;
constexpr int L_TAG = L_ML + 16;
constexpr int L_END = L_TAG + 4;

__global__ void __launch_bounds__(NTHR, 2) mha2_decode_kernel(Params prm) {
  __shared__ __attribute__((aligned(16))) float lds[L_END];
  ST(0);
  const int tid = threadIdx.x, lane = tid & 63;
  const int w = __builtin_amdgcn_readfirstlane(tid >> 6);
  const int bid = blockIdx.x;
  const int slot = bid >> 3;
  const int pr = (bid & 7) * 4 + (slot >> 3);
  const int blk = pr >> 4, h = pr & 15;
  const int b = (slot >> 2) & 1, s = slot & 3;
  const int hh = lane >> 5, l32 = lane & 31;
  const int grp = blk * 2 + b, me = s * 16 + h;

  const size_t kvoff = ((size_t)(b * H + h) * SK + s * 1024 + w * 128) * DH * 2;
  const rsrc_t rk = mk_rsrc((const char*)prm.k[blk] + kvoff, NLD * 1024);
  const rsrc_t rv = mk_rsrc((const char*)prm.v[blk] + kvoff, NLD * 1024);
  const unsigned l16 = lane * 16;
  GAS char* ws = (GAS char*)prm.ws;

  v4u ring[WIN];
  v4u wb[32];
#pragma unroll
  for (int i = 0; i < PRE; ++i) ring[i] = bld<AUX_NT>(rk, l16, i * 1024);
  SB();
  const unsigned epoch = __hip_atomic_load((gu32*)(ws + WS_EPOCH + grp * 256), RLX_AGENT);
  v2u xp[7];
  {
    const GAS v2u* xq = (const GAS v2u*)((const GAS char*)prm.x[blk] + b * (DM * 2) + w * 112 + hh * 56);
#pragma unroll
    for (int i = 0; i < 7; ++i) xp[i] = xq[i];
    SB();
    const rsrc_t rq = mk_rsrc((const char*)prm.wq[blk] + (size_t)(w * 56) * (H * DH * 2) + h * (DH * 2), 55 * (H * DH * 2) + DH * 2);
    const unsigned qoff = hh * 28 * (H * DH * 2) + l32 * 16;
#pragma unroll
    for (int i = 0; i < 28; ++i) wb[i] = bld<W_AUX>(rq, qoff, i * (H * DH * 2));
  }
  SB();
#pragma unroll
  for (int i = PRE; i < WIN; ++i) ring[i] = bld<AUX_NT>(rk, l16, i * 1024);
  SB();
  {
    float qa[8];
#pragma unroll
    for (int j = 0; j < 8; ++j) qa[j] = 0.f;
#pragma unroll
    for (int m = 0; m < 14; ++m) {
      const unsigned xv = xp[m >> 1][m & 1];
#pragma unroll
      for (int j = 0; j < 4; ++j) {
        const unsigned a = wb[2 * m][j], c = wb[2 * m + 1][j];
        qa[2 * j] = dot2(__builtin_amdgcn_perm(c, a, 0x05040100u), xv, qa[2 * j]);
        qa[2 * j + 1] = dot2(__builtin_amdgcn_perm(c, a, 0x07060302u), xv, qa[2 * j + 1]);
      }
    }
    f32x4* dst = (f32x4*)(lds + L_QP + (w * 2 + hh) * 256 + l32 * 8);
    dst[0] = f32x4{qa[0], qa[1], qa[2], qa[3]};
    dst[1] = f32x4{qa[4], qa[5], qa[6], qa[7]};
    if (tid == 0) ((unsigned*)lds)[L_TAG] = epoch + 1u;
  }
  ST(1);
  LDS_BARRIER();
  if (tid < 128) {
    float q0 = 0.f, q1 = 0.f;
#pragma unroll
    for (int i = 0; i < 16; ++i) {
      const float2 t = *(const float2*)(lds + L_QP + i * 256 + tid * 2);
      q0 += t.x;
      q1 += t.y;
    }
    ((unsigned*)lds)[L_QB + tid] = pack_bf16(q0, q1);
  }
  LDS_BARRIER();
  const v4u qv = *(const v4u*)(lds + L_QB + l32 * 4);
  ST(2);

  const int rc = lane >> 3, c8 = lane & 7, qd = (lane >> 2) & 1;
  const rsrc_t ro = mk_rsrc((const char*)prm.wo[blk] + (size_t)h * DH * (DM * 2) + w * 112, 255 * (DM * 2) + 112);
  const int cc = c8 < 7 ? c8 : 6;
  const unsigned woA = (unsigned)((32 * rc + 16 * qd) * (DM * 2) + cc * 16);
  const unsigned woB = (unsigned)((32 * rc + 16 * (1 - qd)) * (DM * 2) + cc * 16);

  float sc[2];
  {
    float xs[16];
    float half_red[2];
#pragma unroll
    for (int t = 0; t < NLD; ++t) {
      const v4u cur = ring[t % WIN];
      float d = dot2(cur[0], qv[0], 0.f);
      d = dot2(cur[1], qv[1], d);
      d = dot2(cur[2], qv[2], d);
      d = dot2(cur[3], qv[3], d);
      xs[t & 15] = d;
      SB();
      {
        const int n = t + WIN;
        ring[t % WIN] = n < NLD ? bld<AUX_NT>(rk, l16, n * 1024) : bld<AUX_NT>(rv, l16, (n - NLD) * 1024);
      }
      if (t < 32) wb[t] = bld<W_AUX>(ro, t < 16 ? woA : woB, (t & 15) * (DM * 2));
      SB();
      if ((t & 15) == 15) {
#define TR_STEP(HALF, CTRL, BIT)                                                   \
  _Pragma("unroll") for (int u = 0; u < (HALF); ++u) {                             \
    const float lo_ = xs[u], hi_ = xs[u + (HALF)];                                 \
    const float t1_ = lo_ + dppf<CTRL>(lo_), t2_ = hi_ + dppf<CTRL>(hi_);          \
    xs[u] = (lane & (BIT)) ? t2_ : t1_;                                            \
  }
        TR_STEP(8, XOR8, 8) TR_STEP(4, XOR7, 4) TR_STEP(2, XOR2, 2) TR_STEP(1, XOR1, 1)
#undef TR_STEP
        half_red[(t >> 4) & 1] = xs[0];
        if ((t & 31) == 31) sc[t >> 5] = swap16_add(half_red[0], half_red[1]);
        SB();
      }
    }
  }
  ST(3);
  constexpr float C2 = 0.0625f * 1.4426950408889634f;
  const float mw = wave_max(fmaxf(sc[0], sc[1])) * C2;
  const float p0 = __builtin_amdgcn_exp2f(fmaf(sc[0], C2, -mw));
  const float p1 = __builtin_amdgcn_exp2f(fmaf(sc[1], C2, -mw));
  const float lw = wave_sum(p0 + p1);
  lds[L_P + w * 128 + hh * 64 + l32] = p0;
  lds[L_P + w * 128 + hh * 64 + 32 + l32] = p1;
  if (lane == 0) {
    lds[L_ML + w] = mw;
    lds[L_ML + 8 + w] = lw;
  }
  asm volatile("s_waitcnt lgkmcnt(0)" ::: "memory");
  SB();

  float acc[8];
#pragma unroll
  for (int j = 0; j < 8; ++j) acc[j] = 0.f;
  {
    const f32x4* pl = (const f32x4*)(lds + L_P + w * 128 + hh * 64);
    f32x4 pc = pl[0];
#pragma unroll
    for (int t = 0; t < NLD; ++t) {
      const v4u cur = ring[(t + NLD) % WIN];
      const float pv = pc[t & 3];
      if ((t & 3) == 3 && t + 1 < NLD) pc = pl[(t + 1) >> 2];
#pragma unroll
      for (int j = 0; j < 4; ++j) {
        acc[2 * j] = fmaf(pv, __uint_as_float(cur[j] << 16), acc[2 * j]);
        acc[2 * j + 1] = fmaf(pv, __uint_as_float(cur[j] & 0xffff0000u), acc[2 * j + 1]);
      }
      SB();
      if (t + WIN < NLD) ring[(t + NLD) % WIN] = bld<AUX_NT>(rv, l16, (t + WIN) * 1024);
      SB();
      if (t < 16) {
#pragma unroll
        for (int j = 0; j < 4; ++j) {
          const unsigned a = wb[2 * t][j], c = wb[2 * t + 1][j];
          wb[2 * t][j] = __builtin_amdgcn_perm(c, a, 0x05040100u);
          wb[2 * t + 1][j] = __builtin_amdgcn_perm(c, a, 0x07060302u);
        }
        SB();
      }
    }
  }
  ST(4);
  {
    f32x4 r;
#pragma unroll
    for (int j = 0; j < 4; ++j) r[j] = swap32_add(acc[j], acc[j + 4]);
    *(f32x4*)(lds + L_ACC + w * 256 + l32 * 8 + hh * 4) = r;
  }
  LDS_BARRIER();
  float lse;
  unsigned o0, o1;
  {
    const f32x4 ma = *(const f32x4*)(lds + L_ML), mb = *(const f32x4*)(lds + L_ML + 4);
    const f32x4 la = *(const f32x4*)(lds + L_ML + 8), lb = *(const f32x4*)(lds + L_ML + 12);
    f32x4 a[8];
#pragma unroll
    for (int i = 0; i < 8; ++i) a[i] = *(const f32x4*)(lds + L_ACC + i * 256 + lane * 4);
    const float M = fmaxf(fmaxf(fmaxf(ma[0], ma[1]), fmaxf(ma[2], ma[3])), fmaxf(fmaxf(mb[0], mb[1]), fmaxf(mb[2], mb[3])));
    float e[8];
#pragma unroll
    for (int i = 0; i < 4; ++i) {
      e[i] = __builtin_amdgcn_exp2f(ma[i] - M);
      e[i + 4] = __builtin_amdgcn_exp2f(mb[i] - M);
    }
    float L = 0.f;
#pragma unroll
    for (int i = 0; i < 4; ++i) L = fmaf(e[i], la[i], L);
#pragma unroll
    for (int i = 0; i < 4; ++i) L = fmaf(e[i + 4], lb[i], L);
    f32x4 o = a[0] * e[0];
#pragma unroll
    for (int i = 1; i < 8; ++i) o += a[i] * e[i];
    const float inv = 1.0f / L;
    o *= inv;
    lse = M + __builtin_amdgcn_logf(L);
    o0 = pack_bf16(o[0], o[1]);
    o1 = pack_bf16(o[2], o[3]);
  }
  const unsigned tag = ((const unsigned*)lds)[L_TAG];
  float y[8];
#pragma unroll
  for (int j = 0; j < 8; ++j) y[j] = 0.f;
  {
    const unsigned m0 = dppu<XOR7>(o0), m1 = dppu<XOR7>(o1);
#define WO_STEP(OV, I)                                        \
  {                                                           \
    const unsigned ov_ = (OV);                                \
    _Pragma("unroll") for (int j = 0; j < 4; ++j) {           \
      y[2 * j] = dot2(wb[2 * (I)][j], ov_, y[2 * j]);         \
      y[2 * j + 1] = dot2(wb[2 * (I) + 1][j], ov_, y[2 * j + 1]); \
    }                                                         \
  }
    WO_STEP(dppu<0x00>(o0), 0) WO_STEP(dppu<0x00>(o1), 1)
    WO_STEP(dppu<0x55>(o0), 2) WO_STEP(dppu<0x55>(o1), 3)
    WO_STEP(dppu<0xAA>(o0), 4) WO_STEP(dppu<0xAA>(o1), 5)
    WO_STEP(dppu<0xFF>(o0), 6) WO_STEP(dppu<0xFF>(o1), 7)
    WO_STEP(dppu<0xFF>(m0), 8) WO_STEP(dppu<0xFF>(m1), 9)
    WO_STEP(dppu<0xAA>(m0), 10) WO_STEP(dppu<0xAA>(m1), 11)
    WO_STEP(dppu<0x55>(m0), 12) WO_STEP(dppu<0x55>(m1), 13)
    WO_STEP(dppu<0x00>(m0), 14) WO_STEP(dppu<0x00>(m1), 15)
#undef WO_STEP
  }
  float yv;
  {
    float z[4], u[2];
#pragma unroll
    for (int j = 0; j < 4; ++j) z[j] = swap32_add(y[j], y[j + 4]);
#pragma unroll
    for (int j = 0; j < 2; ++j) u[j] = swap16_add(z[j], z[j + 2]);
    const float t0 = u[0] + dppf<XOR8>(u[0]), t1 = u[1] + dppf<XOR8>(u[1]);
    yv = (lane & 8) ? t1 : t0;
  }
  if (c8 == 7) yv = lse;
  {
    gu64* g = (gu64*)(ws + WS_INBOX + ((size_t)((grp * 64 + w * 8 + rc) * 64 + me) * 64 + c8 * 8));
    __hip_atomic_store(g, ((unsigned long long)tag << 32) | __float_as_uint(yv), RLX_AGENT);
  }
  ST(5);
  if (w != 0) { ST_FLUSH(); return; }

  {
    const rsrc_t rs = mk_rsrc((const char*)prm.ws + WS_INBOX + (size_t)(grp * 64 + me) * 4096, 4096);
    v4u g[4];
    for (unsigned spins = 0;; ++spins) {
#pragma unroll
      for (int t = 0; t < 4; ++t) g[t] = bld<AUX_SC1>(rs, l16, t * 1024);
      bool ok = true;
#pragma unroll
      for (int t = 0; t < 4; ++t) ok = ok && (g[t][1] == tag) && (g[t][3] == tag);
      if (__all(ok) || spins > SPIN_CAP) break;
      __builtin_amdgcn_s_sleep(2);
      asm volatile("" ::: "memory");
    }
    ST(6);
    float ls[4];
#pragma unroll
    for (int t = 0; t < 4; ++t) ls[t] = dppf<0xFF>(__uint_as_float(g[t][2]));
    const float M = fmaxf(fmaxf(ls[0], ls[1]), fmaxf(ls[2], ls[3]));
    float e[4], L = 0.f;
#pragma unroll
    for (int t = 0; t < 4; ++t) {
      e[t] = __builtin_amdgcn_exp2f(ls[t] - M);
      L += e[t];
    }
    float r0 = 0.f, r1 = 0.f;
#pragma unroll
    for (int t = 0; t < 4; ++t) {
      r0 = fmaf(e[t], __uint_as_float(g[t][0]), r0);
      r1 = fmaf(e[t], __uint_as_float(g[t][2]), r1);
    }
    const float inv = 1.0f / L;
    r0 *= inv;
    r1 *= inv;
    r0 += dppf<ROR4>(r0);
    r1 += dppf<ROR4>(r1);
    r0 += dppf<XOR8>(r0);
    r1 += dppf<XOR8>(r1);
    float r = swap16_add(r0, r1);
    r = swap32_add(r, r);
    const int cpos = 2 * (lane & 3) + ((lane >> 4) & 1);
    if ((lane & 44) == 0 && cpos < 7) {
      const int col = 56 * (me >> 3) + 8 * cpos + (me & 7);
      const unsigned short ob = (unsigned short)(pack_bf16(r, r) & 0xffffu);
      __hip_atomic_store((GAS unsigned short*)prm.out + b * (2 * DM) + blk * DM + col, ob, RLX_AGENT);
    }
    if (me == 0 && lane == 0) __hip_atomic_store((gu32*)(ws + WS_EPOCH + grp * 256), tag, RLX_AGENT);
    ST(7);
    ST_FLUSH();
  }
}

extern "C" void kernel_launch(void* const* d_in, const int* in_sizes, int n_in, void* d_out, int out_size, void* d_ws,
                              size_t ws_size, hipStream_t stream) {
  Params p;
  p.x[0] = d_in[0];
  p.k[0] = d_in[1];
  p.v[0] = d_in[2];
  p.wq[0] = d_in[3];
  p.wo[0] = d_in[4];
  p.x[1] = d_in[5];
  p.k[1] = d_in[6];
  p.v[1] = d_in[7];
  p.wq[1] = d_in[8];
  p.wo[1] = d_in[9];
  p.out = d_out;
  p.ws = d_ws;
  mha2_decode_kernel<<<dim3(256), dim3(NTHR), 0, stream>>>(p);
}
```

```cpp
#define PROBE 0
#include <hip/hip_runtime.h>
#include <hip/hip_bf16.h>
#include <stdint.h>

#define GAS __attribute__((address_space(1)))
typedef unsigned v4u __attribute__((ext_vector_type(4)));
typedef unsigned v2u __attribute__((ext_vector_type(2)));
typedef float f32x4 __attribute__((ext_vector_type(4)));
typedef __bf16 bf16x2 __attribute__((ext_vector_type(2)));
typedef GAS unsigned gu32;
typedef GAS unsigned long long gu64;
#define RLX_AGENT __ATOMIC_RELAXED, __HIP_MEMORY_SCOPE_AGENT

#define WIN 16
#define PRE 4

constexpr int H = 16, SK = 4096, DH = 256, DM = 448;
constexpr int NWAVE = 8, NTHR = 512;
constexpr int NLD = 64;
constexpr size_t WS_EPOCH = 0;
constexpr size_t WS_INBOX = 4096;
constexpr unsigned SPIN_CAP = 1u << 20;

struct Params {
  const void* x[2];
  const void* k[2];
  const void* v[2];
  const void* wq[2];
  const void* wo[2];
  void* out;
  void* ws;
};

__device__ __forceinline__ float dot2(unsigned a, unsigned b, float c) {
  return __builtin_amdgcn_fdot2_f32_bf16(__builtin_bit_cast(bf16x2, a), __builtin_bit_cast(bf16x2, b), c, false);
}
__device__ __forceinline__ unsigned pack_bf16(float lo, float hi) {
  bf16x2 r;
  r.x = (__bf16)lo;
  r.y = (__bf16)hi;
  return __builtin_bit_cast(unsigned, r);
}
template <int CTRL> __device__ __forceinline__ float dppf(float x) {
  return __builtin_bit_cast(float, __builtin_amdgcn_mov_dpp(__builtin_bit_cast(int, x), CTRL, 0xf, 0xf, true));
}
template <int CTRL> __device__ __forceinline__ unsigned dppu(unsigned x) {
  return (unsigned)__builtin_amdgcn_mov_dpp((int)x, CTRL, 0xf, 0xf, true);
}
constexpr int XOR1 = 0xB1, XOR2 = 0x4E, XOR7 = 0x141, XOR8 = 0x128, ROR4 = 0x124;
__device__ __forceinline__ float swap16_add(float a, float b) {
  auto r = __builtin_amdgcn_permlane16_swap(__float_as_uint(a), __float_as_uint(b), false, false);
  return __uint_as_float(r[0]) + __uint_as_float(r[1]);
}
__device__ __forceinline__ float swap32_add(float a, float b) {
  auto r = __builtin_amdgcn_permlane32_swap(__float_as_uint(a), __float_as_uint(b), false, false);
  return __uint_as_float(r[0]) + __uint_as_float(r[1]);
}
__device__ __forceinline__ float swap16_max(float a) {
  auto r = __builtin_amdgcn_permlane16_swap(__float_as_uint(a), __float_as_uint(a), false, false);
  return fmaxf(__uint_as_float(r[0]), __uint_as_float(r[1]));
}
__device__ __forceinline__ float swap32_max(float a) {
  auto r = __builtin_amdgcn_permlane32_swap(__float_as_uint(a), __float_as_uint(a), false, false);
  return fmaxf(__uint_as_float(r[0]), __uint_as_float(r[1]));
}
__device__ __forceinline__ float wave_max(float v) {
  v = fmaxf(v, dppf<XOR1>(v));
  v = fmaxf(v, dppf<XOR2>(v));
  v = fmaxf(v, dppf<XOR7>(v));
  v = fmaxf(v, dppf<XOR8>(v));
  v = swap16_max(v);
  return swap32_max(v);
}
__device__ __forceinline__ float wave_sum(float v) {
  v += dppf<XOR1>(v);
  v += dppf<XOR2>(v);
  v += dppf<XOR7>(v);
  v += dppf<XOR8>(v);
  v = swap16_add(v, v);
  return swap32_add(v, v);
}
typedef __amdgpu_buffer_rsrc_t rsrc_t;
__device__ __forceinline__ rsrc_t mk_rsrc(const void* base, unsigned bytes) {
  return __builtin_amdgcn_make_buffer_rsrc((void*)base, 0, bytes, 0x00020000);
}
#define KV_AUX 2
#define W_AUX 0
constexpr int AUX_NT = KV_AUX, AUX_SC1 = 16;
template <int AUX> __device__ __forceinline__ v4u bld(rsrc_t rs, unsigned voff, unsigned soff) {
  return __builtin_amdgcn_raw_buffer_load_b128(rs, voff, soff, AUX);
}
#define SB() __builtin_amdgcn_sched_barrier(0)
#define LDS_BARRIER()                                      \
  do {                                                     \
    asm volatile("s_waitcnt lgkmcnt(0)" ::: "memory");     \
    __builtin_amdgcn_s_barrier();                          \
    asm volatile("" ::: "memory");                         \
  } while (0)

#define STAMP
#define STAMP_OFF (64u << 20)
#define ST(k) do { SB(); stamps[k] = __builtin_amdgcn_s_memrealtime(); SB(); } while (0)
#define ST_FLUSH() do { if (lane == 0) { for (int k_ = 0; k_ < 8; ++k_) ((GAS unsigned long long*)(ws + STAMP_OFF))[(bid * 8 + w) * 8 + k_] = stamps[k_]; } } while (0)
constexpr int L_QP = 0;
constexpr int L_QB = L_QP + 16 * 256;
constexpr int L_P = L_QB + 128;
constexpr int L_ACC = L_P + 8 * 128;
constexpr int L_ML = L_ACC + 8 * 256;
constexpr int L_TAG = L_ML + 16;
constexpr int L_END = L_TAG + 4;

__global__ void __launch_bounds__(NTHR, 2) mha2_decode_kernel(Params prm) {
  __shared__ __attribute__((aligned(16))) float lds[L_END];
  unsigned long long stamps[8] = {0, 0, 0, 0, 0, 0, 0, 0};
  ST(0);
  const int tid = threadIdx.x, lane = tid & 63;
  const int w = __builtin_amdgcn_readfirstlane(tid >> 6);
  const int bid = blockIdx.x;
  const int slot = bid >> 3;
  const int pr = (bid & 7) * 4 + (slot >> 3);
  const int blk = pr >> 4, h = pr & 15;
  const int b = (slot >> 2) & 1, s = slot & 3;
  const int hh = lane >> 5, l32 = lane & 31;
  const int grp = blk * 2 + b, me = s * 16 + h;

  const size_t kvoff = ((size_t)(b * H + h) * SK + s * 1024 + w * 128) * DH * 2;
  const rsrc_t rk = mk_rsrc((const char*)prm.k[blk] + kvoff, NLD * 1024);
  const rsrc_t rv = mk_rsrc((const char*)prm.v[blk] + kvoff, NLD * 1024);
  const unsigned l16 = lane * 16;
  GAS char* ws = (GAS char*)prm.ws;

  v4u ring[WIN];
  v4u wb[32];
#pragma unroll
  for (int i = 0; i < PRE; ++i) ring[i] = bld<AUX_NT>(rk, l16, i * 1024);
  SB();
  const unsigned epoch = __hip_atomic_load((gu32*)(ws + WS_EPOCH + grp * 256), RLX_AGENT);
  v2u xp[7];
  {
    const GAS v2u* xq = (const GAS v2u*)((const GAS char*)prm.x[blk] + b * (DM * 2) + w * 112 + hh * 56);
#pragma unroll
    for (int i = 0; i < 7; ++i) xp[i] = xq[i];
    SB();
    const rsrc_t rq = mk_rsrc((const char*)prm.wq[blk] + (size_t)(w * 56) * (H * DH * 2) + h * (DH * 2), 55 * (H * DH * 2) + DH * 2);
    const unsigned qoff = hh * 28 * (H * DH * 2) + l32 * 16;
#pragma unroll
    for (int i = 0; i < 28; ++i) wb[i] = bld<W_AUX>(rq, qoff, i * (H * DH * 2));
  }
  SB();
#pragma unroll
  for (int i = PRE; i < WIN; ++i) ring[i] = bld<AUX_NT>(rk, l16, i * 1024);
  SB();
  {
    float qa[8];
#pragma unroll
    for (int j = 0; j < 8; ++j) qa[j] = 0.f;
#pragma unroll
    for (int m = 0; m < 14; ++m) {
      const unsigned xv = xp[m >> 1][m & 1];
#pragma unroll
      for (int j = 0; j < 4; ++j) {
        const unsigned a = wb[2 * m][j], c = wb[2 * m + 1][j];
        qa[2 * j] = dot2(__builtin_amdgcn_perm(c, a, 0x05040100u), xv, qa[2 * j]);
        qa[2 * j + 1] = dot2(__builtin_amdgcn_perm(c, a, 0x07060302u), xv, qa[2 * j + 1]);
      }
    }
    f32x4* dst = (f32x4*)(lds + L_QP + (w * 2 + hh) * 256 + l32 * 8);
    dst[0] = f32x4{qa[0], qa[1], qa[2], qa[3]};
    dst[1] = f32x4{qa[4], qa[5], qa[6], qa[7]};
    if (tid == 0) ((unsigned*)lds)[L_TAG] = epoch + 1u;
  }
  ST(1);
  LDS_BARRIER();
  if (tid < 128) {
    float q0 = 0.f, q1 = 0.f;
#pragma unroll
    for (int i = 0; i < 16; ++i) {
      const float2 t = *(const float2*)(lds + L_QP + i * 256 + tid * 2);
      q0 += t.x;
      q1 += t.y;
    }
    ((unsigned*)lds)[L_QB + tid] = pack_bf16(q0, q1);
  }
  LDS_BARRIER();
  const v4u qv = *(const v4u*)(lds + L_QB + l32 * 4);
  ST(2);

  const int rc = lane >> 3, c8 = lane & 7, qd = (lane >> 2) & 1;
  const rsrc_t ro = mk_rsrc((const char*)prm.wo[blk] + (size_t)h * DH * (DM * 2) + w * 112, 255 * (DM * 2) + 112);
  const int cc = c8 < 7 ? c8 : 6;
  const unsigned woA = (unsigned)((32 * rc + 16 * qd) * (DM * 2) + cc * 16);
  const unsigned woB = (unsigned)((32 * rc + 16 * (1 - qd)) * (DM * 2) + cc * 16);

  float sc[2];
  {
    float xs[16];
    float half_red[2];
#pragma unroll
    for (int t = 0; t < NLD; ++t) {
      const v4u cur = ring[t % WIN];
      float d = dot2(cur[0], qv[0], 0.f);
      d = dot2(cur[1], qv[1], d);
      d = dot2(cur[2], qv[2], d);
      d = dot2(cur[3], qv[3], d);
      xs[t & 15] = d;
      SB();
      {
        const int n = t + WIN;
        ring[t % WIN] = n < NLD ? bld<AUX_NT>(rk, l16, n * 1024) : bld<AUX_NT>(rv, l16, (n - NLD) * 1024);
      }
      if (t < 32) wb[t] = bld<W_AUX>(ro, t < 16 ? woA : woB, (t & 15) * (DM * 2));
      SB();
      if ((t & 15) == 15) {
#define TR_STEP(HALF, CTRL, BIT)                                                   \
  _Pragma("unroll") for (int u = 0; u < (HALF); ++u) {                             \
    const float lo_ = xs[u], hi_ = xs[u + (HALF)];                                 \
    const float t1_ = lo_ + dppf<CTRL>(lo_), t2_ = hi_ + dppf<CTRL>(hi_);          \
    xs[u] = (lane & (BIT)) ? t2_ : t1_;                                            \
  }
        TR_STEP(8, XOR8, 8) TR_STEP(4, XOR7, 4) TR_STEP(2, XOR2, 2) TR_STEP(1, XOR1, 1)
#undef TR_STEP
        half_red[(t >> 4) & 1] = xs[0];
        if ((t & 31) == 31) sc[t >> 5] = swap16_add(half_red[0], half_red[1]);
        SB();
      }
    }
  }
  ST(3);
  constexpr float C2 = 0.0625f * 1.4426950408889634f;
  const float mw = wave_max(fmaxf(sc[0], sc[1])) * C2;
  const float p0 = __builtin_amdgcn_exp2f(fmaf(sc[0], C2, -mw));
  const float p1 = __builtin_amdgcn_exp2f(fmaf(sc[1], C2, -mw));
  const float lw = wave_sum(p0 + p1);
  lds[L_P + w * 128 + hh * 64 + l32] = p0;
  lds[L_P + w * 128 + hh * 64 + 32 + l32] = p1;
  if (lane == 0) {
    lds[L_ML + w] = mw;
    lds[L_ML + 8 + w] = lw;
  }
  asm volatile("s_waitcnt lgkmcnt(0)" ::: "memory");
  SB();

  float acc[8];
#pragma unroll
  for (int j = 0; j < 8; ++j) acc[j] = 0.f;
  {
    const f32x4* pl = (const f32x4*)(lds + L_P + w * 128 + hh * 64);
    f32x4 pc = pl[0];
#pragma unroll
    for (int t = 0; t < NLD; ++t) {
      const v4u cur = ring[(t + NLD) % WIN];
      const float pv = pc[t & 3];
      if ((t & 3) == 3 && t + 1 < NLD) pc = pl[(t + 1) >> 2];
#pragma unroll
      for (int j = 0; j < 4; ++j) {
        acc[2 * j] = fmaf(pv, __uint_as_float(cur[j] << 16), acc[2 * j]);
        acc[2 * j + 1] = fmaf(pv, __uint_as_float(cur[j] & 0xffff0000u), acc[2 * j + 1]);
      }
      SB();
      if (t + WIN < NLD) ring[(t + NLD) % WIN] = bld<AUX_NT>(rv, l16, (t + WIN) * 1024);
      SB();
      if (t < 16) {
#pragma unroll
        for (int j = 0; j < 4; ++j) {
          const unsigned a = wb[2 * t][j], c = wb[2 * t + 1][j];
          wb[2 * t][j] = __builtin_amdgcn_perm(c, a, 0x05040100u);
          wb[2 * t + 1][j] = __builtin_amdgcn_perm(c, a, 0x07060302u);
        }
        SB();
      }
    }
  }
  ST(4);
  {
    f32x4 r;
#pragma unroll
    for (int j = 0; j < 4; ++j) r[j] = swap32_add(acc[j], acc[j + 4]);
    *(f32x4*)(lds + L_ACC + w * 256 + l32 * 8 + hh * 4) = r;
  }
  LDS_BARRIER();
  float lse;
  unsigned o0, o1;
  {
    const f32x4 ma = *(const f32x4*)(lds + L_ML), mb = *(const f32x4*)(lds + L_ML + 4);
    const f32x4 la = *(const f32x4*)(lds + L_ML + 8), lb = *(const f32x4*)(lds + L_ML + 12);
    f32x4 a[8];
#pragma unroll
    for (int i = 0; i < 8; ++i) a[i] = *(const f32x4*)(lds + L_ACC + i * 256 + lane * 4);
    const float M = fmaxf(fmaxf(fmaxf(ma[0], ma[1]), fmaxf(ma[2], ma[3])), fmaxf(fmaxf(mb[0], mb[1]), fmaxf(mb[2], mb[3])));
    float e[8];
#pragma unroll
    for (int i = 0; i < 4; ++i) {
      e[i] = __builtin_amdgcn_exp2f(ma[i] - M);
      e[i + 4] = __builtin_amdgcn_exp2f(mb[i] - M);
    }
    float L = 0.f;
#pragma unroll
    for (int i = 0; i < 4; ++i) L = fmaf(e[i], la[i], L);
#pragma unroll
    for (int i = 0; i < 4; ++i) L = fmaf(e[i + 4], lb[i], L);
    f32x4 o = a[0] * e[0];
#pragma unroll
    for (int i = 1; i < 8; ++i) o += a[i] * e[i];
    const float inv = 1.0f / L;
    o *= inv;
    lse = M + __builtin_amdgcn_logf(L);
    o0 = pack_bf16(o[0], o[1]);
    o1 = pack_bf16(o[2], o[3]);
  }
  const unsigned tag = ((const unsigned*)lds)[L_TAG];
  float y[8];
#pragma unroll
  for (int j = 0; j < 8; ++j) y[j] = 0.f;
  {
    const unsigned m0 = dppu<XOR7>(o0), m1 = dppu<XOR7>(o1);
#define WO_STEP(OV, I)                                        \
  {                                                           \
    const unsigned ov_ = (OV);                                \
    _Pragma("unroll") for (int j = 0; j < 4; ++j) {           \
      y[2 * j] = dot2(wb[2 * (I)][j], ov_, y[2 * j]);         \
      y[2 * j + 1] = dot2(wb[2 * (I) + 1][j], ov_, y[2 * j + 1]); \
    }                                                         \
  }
    WO_STEP(dppu<0x00>(o0), 0) WO_STEP(dppu<0x00>(o1), 1)
    WO_STEP(dppu<0x55>(o0), 2) WO_STEP(dppu<0x55>(o1), 3)
    WO_STEP(dppu<0xAA>(o0), 4) WO_STEP(dppu<0xAA>(o1), 5)
    WO_STEP(dppu<0xFF>(o0), 6) WO_STEP(dppu<0xFF>(o1), 7)
    WO_STEP(dppu<0xFF>(m0), 8) WO_STEP(dppu<0xFF>(m1), 9)
    WO_STEP(dppu<0xAA>(m0), 10) WO_STEP(dppu<0xAA>(m1), 11)
    WO_STEP(dppu<0x55>(m0), 12) WO_STEP(dppu<0x55>(m1), 13)
    WO_STEP(dppu<0x00>(m0), 14) WO_STEP(dppu<0x00>(m1), 15)
#undef WO_STEP
  }
  float yv;
  {
    float z[4], u[2];
#pragma unroll
    for (int j = 0; j < 4; ++j) z[j] = swap32_add(y[j], y[j + 4]);
#pragma unroll
    for (int j = 0; j < 2; ++j) u[j] = swap16_add(z[j], z[j + 2]);
    const float t0 = u[0] + dppf<XOR8>(u[0]), t1 = u[1] + dppf<XOR8>(u[1]);
    yv = (lane & 8) ? t1 : t0;
  }
  if (c8 == 7) yv = lse;
  {
    gu64* g = (gu64*)(ws + WS_INBOX + ((size_t)((grp * 64 + w * 8 + rc) * 64 + me) * 64 + c8 * 8));
    __hip_atomic_store(g, ((unsigned long long)tag << 32) | __float_as_uint(yv), RLX_AGENT);
  }
  ST(5);
  if (w != 0) { ST_FLUSH(); return; }

  {
    const rsrc_t rs = mk_rsrc((const char*)prm.ws + WS_INBOX + (size_t)(grp * 64 + me) * 4096, 4096);
    v4u g[4];
    for (unsigned spins = 0;; ++spins) {
#pragma unroll
      for (int t = 0; t < 4; ++t) g[t] = bld<AUX_SC1>(rs, l16, t * 1024);
      bool ok = true;
#pragma unroll
      for (int t = 0; t < 4; ++t) ok = ok && (g[t][1] == tag) && (g[t][3] == tag);
      if (__all(ok) || spins > SPIN_CAP) break;
      __builtin_amdgcn_s_sleep(2);
      asm volatile("" ::: "memory");
    }
    ST(6);
    float ls[4];
#pragma unroll
    for (int t = 0; t < 4; ++t) ls[t] = dppf<0xFF>(__uint_as_float(g[t][2]));
    const float M = fmaxf(fmaxf(ls[0], ls[1]), fmaxf(ls[2], ls[3]));
    float e[4], L = 0.f;
#pragma unroll
    for (int t = 0; t < 4; ++t) {
      e[t] = __builtin_amdgcn_exp2f(ls[t] - M);
      L += e[t];
    }
    float r0 = 0.f, r1 = 0.f;
#pragma unroll
    for (int t = 0; t < 4; ++t) {
      r0 = fmaf(e[t], __uint_as_float(g[t][0]), r0);
      r1 = fmaf(e[t], __uint_as_float(g[t][2]), r1);
    }
    const float inv = 1.0f / L;
    r0 *= inv;
    r1 *= inv;
    r0 += dppf<ROR4>(r0);
    r1 += dppf<ROR4>(r1);
    r0 += dppf<XOR8>(r0);
    r1 += dppf<XOR8>(r1);
    float r = swap16_add(r0, r1);
    r = swap32_add(r, r);
    const int cpos = 2 * (lane & 3) + ((lane >> 4) & 1);
    if ((lane & 44) == 0 && cpos < 7) {
      const int col = 56 * (me >> 3) + 8 * cpos + (me & 7);
      const unsigned short ob = (unsigned short)(pack_bf16(r, r) & 0xffffu);
      __hip_atomic_store((GAS unsigned short*)prm.out + b * (2 * DM) + blk * DM + col, ob, RLX_AGENT);
    }
    if (me == 0 && lane == 0) __hip_atomic_store((gu32*)(ws + WS_EPOCH + grp * 256), tag, RLX_AGENT);
    ST(7);
    { const unsigned long long tgt = stamps[PROBE] + 10000ull; while (__builtin_amdgcn_s_memrealtime() < tgt) __builtin_amdgcn_s_sleep(1); }
    ST_FLUSH();
  }
}

extern "C" void kernel_launch(void* const* d_in, const int* in_sizes, int n_in, void* d_out, int out_size, void* d_ws,
                              size_t ws_size, hipStream_t stream) {
  Params p;
  p.x[0] = d_in[0];
  p.k[0] = d_in[1];
  p.v[0] = d_in[2];
  p.wq[0] = d_in[3];
  p.wo[0] = d_in[4];
  p.x[1] = d_in[5];
  p.k[1] = d_in[6];
  p.v[1] = d_in[7];
  p.wq[1] = d_in[8];
  p.wo[1] = d_in[9];
  p.out = d_out;
  p.ws = d_ws;
  mha2_decode_kernel<<<dim3(256), dim3(NTHR), 0, stream>>>(p);
}
```

```cpp
#define PROBE2 3
#include <hip/hip_runtime.h>
#include <hip/hip_bf16.h>
#include <stdint.h>

#define GAS __attribute__((address_space(1)))
typedef unsigned v4u __attribute__((ext_vector_type(4)));
typedef unsigned v2u __attribute__((ext_vector_type(2)));
typedef float f32x4 __attribute__((ext_vector_type(4)));
typedef __bf16 bf16x2 __attribute__((ext_vector_type(2)));
typedef GAS unsigned gu32;
typedef GAS unsigned long long gu64;
#define RLX_AGENT __ATOMIC_RELAXED, __HIP_MEMORY_SCOPE_AGENT

#define WIN 16
#define PRE 4

constexpr int H = 16, SK = 4096, DH = 256, DM = 448;
constexpr int NWAVE = 8, NTHR = 512;
constexpr int NLD = 64;
constexpr size_t WS_EPOCH = 0;
constexpr size_t WS_INBOX = 4096;
constexpr unsigned SPIN_CAP = 1u << 20;

struct Params {
  const void* x[2];
  const void* k[2];
  const void* v[2];
  const void* wq[2];
  const void* wo[2];
  void* out;
  void* ws;
};

__device__ __forceinline__ float dot2(unsigned a, unsigned b, float c) {
  return __builtin_amdgcn_fdot2_f32_bf16(__builtin_bit_cast(bf16x2, a), __builtin_bit_cast(bf16x2, b), c, false);
}
__device__ __forceinline__ unsigned pack_bf16(float lo, float hi) {
  bf16x2 r;
  r.x = (__bf16)lo;
  r.y = (__bf16)hi;
  return __builtin_bit_cast(unsigned, r);
}
template <int CTRL> __device__ __forceinline__ float dppf(float x) {
  return __builtin_bit_cast(float, __builtin_amdgcn_mov_dpp(__builtin_bit_cast(int, x), CTRL, 0xf, 0xf, true));
}
template <int CTRL> __device__ __forceinline__ unsigned dppu(unsigned x) {
  return (unsigned)__builtin_amdgcn_mov_dpp((int)x, CTRL, 0xf, 0xf, true);
}
constexpr int XOR1 = 0xB1, XOR2 = 0x4E, XOR7 = 0x141, XOR8 = 0x128, ROR4 = 0x124;
__device__ __forceinline__ float swap16_add(float a, float b) {
  auto r = __builtin_amdgcn_permlane16_swap(__float_as_uint(a), __float_as_uint(b), false, false);
  return __uint_as_float(r[0]) + __uint_as_float(r[1]);
}
__device__ __forceinline__ float swap32_add(float a, float b) {
  auto r = __builtin_amdgcn_permlane32_swap(__float_as_uint(a), __float_as_uint(b), false, false);
  return __uint_as_float(r[0]) + __uint_as_float(r[1]);
}
__device__ __forceinline__ float swap16_max(float a) {
  auto r = __builtin_amdgcn_permlane16_swap(__float_as_uint(a), __float_as_uint(a), false, false);
  return fmaxf(__uint_as_float(r[0]), __uint_as_float(r[1]));
}
__device__ __forceinline__ float swap32_max(float a) {
  auto r = __builtin_amdgcn_permlane32_swap(__float_as_uint(a), __float_as_uint(a), false, false);
  return fmaxf(__uint_as_float(r[0]), __uint_as_float(r[1]));
}
__device__ __forceinline__ float wave_max(float v) {
  v = fmaxf(v, dppf<XOR1>(v));
  v = fmaxf(v, dppf<XOR2>(v));
  v = fmaxf(v, dppf<XOR7>(v));
  v = fmaxf(v, dppf<XOR8>(v));
  v = swap16_max(v);
  return swap32_max(v);
}
__device__ __forceinline__ float wave_sum(float v) {
  v += dppf<XOR1>(v);
  v += dppf<XOR2>(v);
  v += dppf<XOR7>(v);
  v += dppf<XOR8>(v);
  v = swap16_add(v, v);
  return swap32_add(v, v);
}
typedef __amdgpu_buffer_rsrc_t rsrc_t;
__device__ __forceinline__ rsrc_t mk_rsrc(const void* base, unsigned bytes) {
  return __builtin_amdgcn_make_buffer_rsrc((void*)base, 0, bytes, 0x00020000);
}
#define KV_AUX 2
#define W_AUX 0
constexpr int AUX_NT = KV_AUX, AUX_SC1 = 16;
template <int AUX> __device__ __forceinline__ v4u bld(rsrc_t rs, unsigned voff, unsigned soff) {
  return __builtin_amdgcn_raw_buffer_load_b128(rs, voff, soff, AUX);
}
#define SB() __builtin_amdgcn_sched_barrier(0)
#define LDS_BARRIER()                                      \
  do {                                                     \
    asm volatile("s_waitcnt lgkmcnt(0)" ::: "memory");     \
    __builtin_amdgcn_s_barrier();                          \
    asm volatile("" ::: "memory");                         \
  } while (0)

#define STAMP
#define STAMP_OFF (64u << 20)
#define ST(k) do { SB(); stamps[k] = __builtin_amdgcn_s_memrealtime(); SB(); } while (0)
#define ST_FLUSH() do { if (lane == 0) { for (int k_ = 0; k_ < 8; ++k_) ((GAS unsigned long long*)(ws + STAMP_OFF))[(bid * 8 + w) * 8 + k_] = stamps[k_]; } } while (0)
constexpr int L_QP = 0;
constexpr int L_QB = L_QP + 16 * 256;
constexpr int L_P = L_QB + 128;
constexpr int L_ACC = L_P + 8 * 128;
constexpr int L_ML = L_ACC + 8 * 256;
constexpr int L_TAG = L_ML + 16;
constexpr int L_END = L_TAG + 4;

__global__ void __launch_bounds__(NTHR, 2) mha2_decode_kernel(Params prm) {
  __shared__ __attribute__((aligned(16))) float lds[L_END];
  unsigned long long stamps[8] = {0, 0, 0, 0, 0, 0, 0, 0};
  ST(0);
  const int tid = threadIdx.x, lane = tid & 63;
  const int w = __builtin_amdgcn_readfirstlane(tid >> 6);
  const int bid = blockIdx.x;
  const int slot = bid >> 3;
  const int pr = (bid & 7) * 4 + (slot >> 3);
  const int blk = pr >> 4, h = pr & 15;
  const int b = (slot >> 2) & 1, s = slot & 3;
  const int hh = lane >> 5, l32 = lane & 31;
  const int grp = blk * 2 + b, me = s * 16 + h;

  const size_t kvoff = ((size_t)(b * H + h) * SK + s * 1024 + w * 128) * DH * 2;
  const rsrc_t rk = mk_rsrc((const char*)prm.k[blk] + kvoff, NLD * 1024);
  const rsrc_t rv = mk_rsrc((const char*)prm.v[blk] + kvoff, NLD * 1024);
  const unsigned l16 = lane * 16;
  GAS char* ws = (GAS char*)prm.ws;

  v4u ring[WIN];
  v4u wb[32];
#pragma unroll
  for (int i = 0; i < PRE; ++i) ring[i] = bld<AUX_NT>(rk, l16, i * 1024);
  SB();
  const unsigned epoch = __hip_atomic_load((gu32*)(ws + WS_EPOCH + grp * 256), RLX_AGENT);
  v2u xp[7];
  {
    const GAS v2u* xq = (const GAS v2u*)((const GAS char*)prm.x[blk] + b * (DM * 2) + w * 112 + hh * 56);
#pragma unroll
    for (int i = 0; i < 7; ++i) xp[i] = xq[i];
    SB();
    const rsrc_t rq = mk_rsrc((const char*)prm.wq[blk] + (size_t)(w * 56) * (H * DH * 2) + h * (DH * 2), 55 * (H * DH * 2) + DH * 2);
    const unsigned qoff = hh * 28 * (H * DH * 2) + l32 * 16;
#pragma unroll
    for (int i = 0; i < 28; ++i)
      wb[i] = bld<W_AUX>(rq, qoff, i * (H * DH * 2));
  }
  SB();
#pragma unroll
  for (int i = PRE; i < WIN; ++i) ring[i] = bld<AUX_NT>(rk, l16, i * 1024);
  SB();
  {
    float qa[8];
#pragma unroll
    for (int j = 0; j < 8; ++j) qa[j] = 0.f;
#pragma unroll
    for (int m = 0; m < 14; ++m) {
      const unsigned xv = xp[m >> 1][m & 1];
#pragma unroll
      for (int j = 0; j < 4; ++j) {
        const unsigned a = wb[2 * m][j], c = wb[2 * m + 1][j];
        qa[2 * j] = dot2(__builtin_amdgcn_perm(c, a, 0x05040100u), xv, qa[2 * j]);
        qa[2 * j + 1] = dot2(__builtin_amdgcn_perm(c, a, 0x07060302u), xv, qa[2 * j + 1]);
      }
    }
    f32x4* dst = (f32x4*)(lds + L_QP + (w * 2 + hh) * 256 + l32 * 8);
    dst[0] = f32x4{qa[0], qa[1], qa[2], qa[3]};
    dst[1] = f32x4{qa[4], qa[5], qa[6], qa[7]};
    if (tid == 0) ((unsigned*)lds)[L_TAG] = epoch + 1u;
  }
  ST(1);
  LDS_BARRIER();
  if (tid < 128) {
    float q0 = 0.f, q1 = 0.f;
#pragma unroll
    for (int i = 0; i < 16; ++i) {
      const float2 t = *(const float2*)(lds + L_QP + i * 256 + tid * 2);
      q0 += t.x;
      q1 += t.y;
    }
    ((unsigned*)lds)[L_QB + tid] = pack_bf16(q0, q1);
  }
  LDS_BARRIER();
  const v4u qv = *(const v4u*)(lds + L_QB + l32 * 4);
  ST(2);

  const int rc = lane >> 3, c8 = lane & 7, qd = (lane >> 2) & 1;
  const rsrc_t ro = mk_rsrc((const char*)prm.wo[blk] + (size_t)h * DH * (DM * 2) + w * 112, 255 * (DM * 2) + 112);
  const int cc = c8 < 7 ? c8 : 6;
  const unsigned woA = (unsigned)((32 * rc + 16 * qd) * (DM * 2) + cc * 16);
  const unsigned woB = (unsigned)((32 * rc + 16 * (1 - qd)) * (DM * 2) + cc * 16);

  float sc[2];
  {
    float xs[16];
    float half_red[2];
#pragma unroll
    for (int t = 0; t < NLD; ++t) {
      const v4u cur = ring[t % WIN];
      float d = dot2(cur[0], qv[0], 0.f);
      d = dot2(cur[1], qv[1], d);
      d = dot2(cur[2], qv[2], d);
      d = dot2(cur[3], qv[3], d);
      xs[t & 15] = d;
      SB();
      {
        const int n = t + WIN;
        ring[t % WIN] = n < NLD ? bld<AUX_NT>(rk, l16, n * 1024) : bld<AUX_NT>(rv, l16, (n - NLD) * 1024);
      }
      if (t < 32) wb[t] = bld<W_AUX>(ro, t < 16 ? woA : woB, (t & 15) * (DM * 2));
      SB();
      if ((t & 15) == 15) {
#define TR_STEP(HALF, CTRL, BIT)                                                   \
  _Pragma("unroll") for (int u = 0; u < (HALF); ++u) {                             \
    const float lo_ = xs[u], hi_ = xs[u + (HALF)];                                 \
    const float t1_ = lo_ + dppf<CTRL>(lo_), t2_ = hi_ + dppf<CTRL>(hi_);          \
    xs[u] = (lane & (BIT)) ? t2_ : t1_;                                            \
  }
        TR_STEP(8, XOR8, 8) TR_STEP(4, XOR7, 4) TR_STEP(2, XOR2, 2) TR_STEP(1, XOR1, 1)
#undef TR_STEP
        half_red[(t >> 4) & 1] = xs[0];
        if ((t & 31) == 31) sc[t >> 5] = swap16_add(half_red[0], half_red[1]);
        SB();
      }
    }
  }
  ST(3);
  constexpr float C2 = 0.0625f * 1.4426950408889634f;
  const float mw = wave_max(fmaxf(sc[0], sc[1])) * C2;
  const float p0 = __builtin_amdgcn_exp2f(fmaf(sc[0], C2, -mw));
  const float p1 = __builtin_amdgcn_exp2f(fmaf(sc[1], C2, -mw));
  const float lw = wave_sum(p0 + p1);
  lds[L_P + w * 128 + hh * 64 + l32] = p0;
  lds[L_P + w * 128 + hh * 64 + 32 + l32] = p1;
  if (lane == 0) {
    lds[L_ML + w] = mw;
    lds[L_ML + 8 + w] = lw;
  }
  asm volatile("s_waitcnt lgkmcnt(0)" ::: "memory");
  SB();

  float acc[8];
#pragma unroll
  for (int j = 0; j < 8; ++j) acc[j] = 0.f;
  {
    const f32x4* pl = (const f32x4*)(lds + L_P + w * 128 + hh * 64);
    f32x4 pc = pl[0];
#pragma unroll
    for (int t = 0; t < NLD; ++t) {
      const v4u cur = ring[(t + NLD) % WIN];
      const float pv = pc[t & 3];
      if ((t & 3) == 3 && t + 1 < NLD) pc = pl[(t + 1) >> 2];
#pragma unroll
      for (int j = 0; j < 4; ++j) {
        acc[2 * j] = fmaf(pv, __uint_as_float(cur[j] << 16), acc[2 * j]);
        acc[2 * j + 1] = fmaf(pv, __uint_as_float(cur[j] & 0xffff0000u), acc[2 * j + 1]);
      }
      SB();
      if (t + WIN < NLD) ring[(t + NLD) % WIN] = bld<AUX_NT>(rv, l16, (t + WIN) * 1024);
      SB();
      if (t < 16) {
#pragma unroll
        for (int j = 0; j < 4; ++j) {
          const unsigned a = wb[2 * t][j], c = wb[2 * t + 1][j];
          wb[2 * t][j] = __builtin_amdgcn_perm(c, a, 0x05040100u);
          wb[2 * t + 1][j] = __builtin_amdgcn_perm(c, a, 0x07060302u);
        }
        SB();
      }
    }
  }
  ST(4);
  {
    f32x4 r;
#pragma unroll
    for (int j = 0; j < 4; ++j) r[j] = swap32_add(acc[j], acc[j + 4]);
    *(f32x4*)(lds + L_ACC + w * 256 + l32 * 8 + hh * 4) = r;
  }
  LDS_BARRIER();
  float lse;
  unsigned o0, o1;
  {
    const f32x4 ma = *(const f32x4*)(lds + L_ML), mb = *(const f32x4*)(lds + L_ML + 4);
    const f32x4 la = *(const f32x4*)(lds + L_ML + 8), lb = *(const f32x4*)(lds + L_ML + 12);
    f32x4 a[8];
#pragma unroll
    for (int i = 0; i < 8; ++i) a[i] = *(const f32x4*)(lds + L_ACC + i * 256 + lane * 4);
    const float M = fmaxf(fmaxf(fmaxf(ma[0], ma[1]), fmaxf(ma[2], ma[3])), fmaxf(fmaxf(mb[0], mb[1]), fmaxf(mb[2], mb[3])));
    float e[8];
#pragma unroll
    for (int i = 0; i < 4; ++i) {
      e[i] = __builtin_amdgcn_exp2f(ma[i] - M);
      e[i + 4] = __builtin_amdgcn_exp2f(mb[i] - M);
    }
    float L = 0.f;
#pragma unroll
    for (int i = 0; i < 4; ++i) L = fmaf(e[i], la[i], L);
#pragma unroll
    for (int i = 0; i < 4; ++i) L = fmaf(e[i + 4], lb[i], L);
    f32x4 o = a[0] * e[0];
#pragma unroll
    for (int i = 1; i < 8; ++i) o += a[i] * e[i];
    const float inv = 1.0f / L;
    o *= inv;
    lse = M + __builtin_amdgcn_logf(L);
    o0 = pack_bf16(o[0], o[1]);
    o1 = pack_bf16(o[2], o[3]);
  }
  const unsigned tag = ((const unsigned*)lds)[L_TAG];
  float y[8];
#pragma unroll
  for (int j = 0; j < 8; ++j) y[j] = 0.f;
  {
    const unsigned m0 = dppu<XOR7>(o0), m1 = dppu<XOR7>(o1);
#define WO_STEP(OV, I)                                        \
  {                                                           \
    const unsigned ov_ = (OV);                                \
    _Pragma("unroll") for (int j = 0; j < 4; ++j) {           \
      y[2 * j] = dot2(wb[2 * (I)][j], ov_, y[2 * j]);         \
      y[2 * j + 1] = dot2(wb[2 * (I) + 1][j], ov_, y[2 * j + 1]); \
    }                                                         \
  }
    WO_STEP(dppu<0x00>(o0), 0) WO_STEP(dppu<0x00>(o1), 1)
    WO_STEP(dppu<0x55>(o0), 2) WO_STEP(dppu<0x55>(o1), 3)
    WO_STEP(dppu<0xAA>(o0), 4) WO_STEP(dppu<0xAA>(o1), 5)
    WO_STEP(dppu<0xFF>(o0), 6) WO_STEP(dppu<0xFF>(o1), 7)
    WO_STEP(dppu<0xFF>(m0), 8) WO_STEP(dppu<0xFF>(m1), 9)
    WO_STEP(dppu<0xAA>(m0), 10) WO_STEP(dppu<0xAA>(m1), 11)
    WO_STEP(dppu<0x55>(m0), 12) WO_STEP(dppu<0x55>(m1), 13)
    WO_STEP(dppu<0x00>(m0), 14) WO_STEP(dppu<0x00>(m1), 15)
#undef WO_STEP
  }
  float yv;
  {
    float z[4], u[2];
#pragma unroll
    for (int j = 0; j < 4; ++j) z[j] = swap32_add(y[j], y[j + 4]);
#pragma unroll
    for (int j = 0; j < 2; ++j) u[j] = swap16_add(z[j], z[j + 2]);
    const float t0 = u[0] + dppf<XOR8>(u[0]), t1 = u[1] + dppf<XOR8>(u[1]);
    yv = (lane & 8) ? t1 : t0;
  }
  if (c8 == 7) yv = lse;
  {
    gu64* g = (gu64*)(ws + WS_INBOX + ((size_t)((grp * 64 + w * 8 + rc) * 64 + me) * 64 + c8 * 8));
    __hip_atomic_store(g, ((unsigned long long)tag << 32) | __float_as_uint(yv), RLX_AGENT);
  }
  ST(5);
  if (w == 0 && lane == 0) __hip_atomic_store((gu64*)(ws + (32u << 20)) + grp * 64 + me, ((unsigned long long)tag << 32) | (unsigned)stamps[0], RLX_AGENT);
  if (w != 0) { ST_FLUSH(); return; }

  {
    const rsrc_t rs = mk_rsrc((const char*)prm.ws + WS_INBOX + (size_t)(grp * 64 + me) * 4096, 4096);
    v4u g[4];
    for (unsigned spins = 0;; ++spins) {
#pragma unroll
      for (int t = 0; t < 4; ++t) g[t] = bld<AUX_SC1>(rs, l16, t * 1024);
      bool ok = true;
#pragma unroll
      for (int t = 0; t < 4; ++t) ok = ok && (g[t][1] == tag) && (g[t][3] == tag);
      if (__all(ok) || spins > SPIN_CAP) break;
      __builtin_amdgcn_s_sleep(2);
      asm volatile("" ::: "memory");
    }
    ST(6);
    float ls[4];
#pragma unroll
    for (int t = 0; t < 4; ++t) ls[t] = dppf<0xFF>(__uint_as_float(g[t][2]));
    const float M = fmaxf(fmaxf(ls[0], ls[1]), fmaxf(ls[2], ls[3]));
    float e[4], L = 0.f;
#pragma unroll
    for (int t = 0; t < 4; ++t) {
      e[t] = __builtin_amdgcn_exp2f(ls[t] - M);
      L += e[t];
    }
    float r0 = 0.f, r1 = 0.f;
#pragma unroll
    for (int t = 0; t < 4; ++t) {
      r0 = fmaf(e[t], __uint_as_float(g[t][0]), r0);
      r1 = fmaf(e[t], __uint_as_float(g[t][2]), r1);
    }
    const float inv = 1.0f / L;
    r0 *= inv;
    r1 *= inv;
    r0 += dppf<ROR4>(r0);
    r1 += dppf<ROR4>(r1);
    r0 += dppf<XOR8>(r0);
    r1 += dppf<XOR8>(r1);
    float r = swap16_add(r0, r1);
    r = swap32_add(r, r);
    const int cpos = 2 * (lane & 3) + ((lane >> 4) & 1);
    if ((lane & 44) == 0 && cpos < 7) {
      const int col = 56 * (me >> 3) + 8 * cpos + (me & 7);
      const unsigned short ob = (unsigned short)(pack_bf16(r, r) & 0xffffu);
      __hip_atomic_store((GAS unsigned short*)prm.out + b * (2 * DM) + blk * DM + col, ob, RLX_AGENT);
    }
    if (me == 0 && lane == 0) __hip_atomic_store((gu32*)(ws + WS_EPOCH + grp * 256), tag, RLX_AGENT);
    ST(7);
    {
      unsigned long long x;
      for (unsigned spins = 0; spins < SPIN_CAP; ++spins) {
        x = __hip_atomic_load((gu64*)(ws + (32u << 20)) + grp * 64 + lane, RLX_AGENT);
        if (__all((unsigned)(x >> 32) == tag)) break;
      }
      float rel = (float)(int)((unsigned)x - (unsigned)stamps[0]);
      const int pxcd = (blk * 16 + (lane & 15)) >> 2;
      float sel;
      if (PROBE2 == 100) sel = -wave_max(-rel);
      else sel = -wave_max(pxcd == (PROBE2 & 7) ? -rel : -1e9f);
      const bool act = (PROBE2 == 100) ? (grp == 0 || grp == 2) : (sel < 1e8f);
      if (act) { const unsigned long long tgt = stamps[0] + (long long)(int)sel + 10000ull; while (__builtin_amdgcn_s_memrealtime() < tgt) __builtin_amdgcn_s_sleep(1); }
    }
    ST_FLUSH();
  }
}

extern "C" void kernel_launch(void* const* d_in, const int* in_sizes, int n_in, void* d_out, int out_size, void* d_ws,
                              size_t ws_size, hipStream_t stream) {
  Params p;
  p.x[0] = d_in[0];
  p.k[0] = d_in[1];
  p.v[0] = d_in[2];
  p.wq[0] = d_in[3];
  p.wo[0] = d_in[4];
  p.x[1] = d_in[5];
  p.k[1] = d_in[6];
  p.v[1] = d_in[7];
  p.wq[1] = d_in[8];
  p.wo[1] = d_in[9];
  p.out = d_out;
  p.ws = d_ws;
  mha2_decode_kernel<<<dim3(256), dim3(NTHR), 0, stream>>>(p);
}
```

```cpp
#include <hip/hip_runtime.h>
#include <hip/hip_bf16.h>
#include <stdint.h>
#include <type_traits>

#define GAS __attribute__((address_space(1)))
#define CAS __attribute__((address_space(4)))
typedef unsigned v2u __attribute__((ext_vector_type(2)));
typedef unsigned v4u __attribute__((ext_vector_type(4)));
typedef unsigned v8u __attribute__((ext_vector_type(8)));
typedef float f32x2 __attribute__((ext_vector_type(2)));
typedef float f32x4 __attribute__((ext_vector_type(4)));
typedef __bf16 bf16x2 __attribute__((ext_vector_type(2)));
typedef __bf16 bf16x8 __attribute__((ext_vector_type(8)));
typedef __bf16 bf16x16 __attribute__((ext_vector_type(16)));
#define RLX_AGENT __ATOMIC_RELAXED, __HIP_MEMORY_SCOPE_AGENT

constexpr int WIN = 16;
constexpr int DIST = 6;
constexpr int FREE_N = 4;
constexpr int N_WQ = 2;
constexpr int N_QCALC = 6;
constexpr int N_QPOLL = 13;
constexpr int WM0 = 6, WMS = 2;
constexpr int WO0 = 12;
constexpr int N_ML = 96;
constexpr int G_ML = 123;
constexpr int PACE_T0 = 55;
constexpr int PACE_DUR = 3490;
constexpr int PACE_GAIN = 128;

constexpr int H = 16, SK = 4096, DH = 256, DM = 448;
constexpr int NTHR = 512;
constexpr int NLD = 64;
constexpr unsigned WS_EPOCH = 0;
constexpr unsigned WS_Q = 4096;
constexpr unsigned WS_ML = WS_Q + 65536;
constexpr unsigned WS_Y = WS_ML + 32768;
constexpr unsigned WS_END = WS_Y + 4 * 14 * 8192;
constexpr unsigned SPIN_CAP = 1u << 20;
constexpr unsigned OOB = 0x7fffff00u;
constexpr int AUX_NT = 2, AUX_SC1 = 16;

struct Params {
  const void* k[2];
  const void* wq[2];
  const void* x[2];
  void* ws;
  void* out;
  const void* v[2];
  const void* wo[2];
};

__device__ __forceinline__ float dot2(unsigned a, unsigned b, float c) {
  return __builtin_amdgcn_fdot2_f32_bf16(__builtin_bit_cast(bf16x2, a), __builtin_bit_cast(bf16x2, b), c, false);
}
__device__ __forceinline__ unsigned pack_bf16(float lo, float hi) {
  bf16x2 r;
  r.x = (__bf16)lo;
  r.y = (__bf16)hi;
  return __builtin_bit_cast(unsigned, r);
}
template <int CTRL> __device__ __forceinline__ float dppf(float x) {
  return __builtin_bit_cast(float, __builtin_amdgcn_mov_dpp(__builtin_bit_cast(int, x), CTRL, 0xf, 0xf, true));
}
template <int CTRL> __device__ __forceinline__ unsigned dppu(unsigned x) {
  return (unsigned)__builtin_amdgcn_mov_dpp((int)x, CTRL, 0xf, 0xf, true);
}
constexpr int XOR1 = 0xB1, XOR2 = 0x4E, XOR7 = 0x141, XOR8 = 0x128, ROR4 = 0x124;
__device__ __forceinline__ float swap16_add(float a, float b) {
  auto r = __builtin_amdgcn_permlane16_swap(__float_as_uint(a), __float_as_uint(b), false, false);
  return __uint_as_float(r[0]) + __uint_as_float(r[1]);
}
__device__ __forceinline__ float swap32_add(float a, float b) {
  auto r = __builtin_amdgcn_permlane32_swap(__float_as_uint(a), __float_as_uint(b), false, false);
  return __uint_as_float(r[0]) + __uint_as_float(r[1]);
}
__device__ __forceinline__ float swap32_max(float a) {
  auto r = __builtin_amdgcn_permlane32_swap(__float_as_uint(a), __float_as_uint(a), false, false);
  return fmaxf(__uint_as_float(r[0]), __uint_as_float(r[1]));
}
__device__ __forceinline__ float half_max(float v) {
  v = fmaxf(v, dppf<XOR1>(v));
  v = fmaxf(v, dppf<XOR2>(v));
  v = fmaxf(v, dppf<XOR7>(v));
  v = fmaxf(v, dppf<XOR8>(v));
  auto r = __builtin_amdgcn_permlane16_swap(__float_as_uint(v), __float_as_uint(v), false, false);
  return fmaxf(__uint_as_float(r[0]), __uint_as_float(r[1]));
}
__device__ __forceinline__ float half_sum(float v) {
  v += dppf<XOR1>(v);
  v += dppf<XOR2>(v);
  v += dppf<XOR7>(v);
  v += dppf<XOR8>(v);
  return swap16_add(v, v);
}
__device__ __forceinline__ float sel(bool c, float a, float b) {
  asm volatile("" : "+v"(a), "+v"(b));
  return c ? a : b;
}
template <int I, int N, class F> __device__ __forceinline__ void static_for(F&& f) {
  if constexpr (I < N) {
    f(std::integral_constant<int, I>{});
    static_for<I + 1, N>(f);
  }
}
#define LAMBDA(v) [&](auto v) __attribute__((always_inline))
typedef __amdgpu_buffer_rsrc_t rsrc_t;
__device__ __forceinline__ rsrc_t mk_rsrc(const void* base, unsigned bytes) {
  return __builtin_amdgcn_make_buffer_rsrc((void*)base, 0, bytes, 0x00020000);
}
template <int AUX> __device__ __forceinline__ v4u bld(rsrc_t rs, unsigned voff, unsigned soff) {
  return __builtin_amdgcn_raw_buffer_load_b128(rs, voff, soff, AUX);
}
#define SB() __builtin_amdgcn_sched_barrier(0)
#define LDS_BARRIER()                                  \
  do {                                                 \
    asm volatile("s_waitcnt lgkmcnt(0)" ::: "memory"); \
    __builtin_amdgcn_s_barrier();                      \
    asm volatile("" ::: "memory");                     \
    SB();                                              \
  } while (0)

constexpr int pace_thr(int n, int depth) {
  const int dith16 = 2 * (((n & 1) << 2) | (n & 2) | ((n & 4) >> 2)) + 1;
  return PACE_T0 + n * PACE_DUR / 128 + (depth == 2 ? -50 : PACE_GAIN * (16 * (depth - 2) - dith16) / 16);
}
template <int N> __device__ __forceinline__ void throttle(unsigned t_start) {
  const int el = (int)((unsigned)__builtin_amdgcn_s_memrealtime() - t_start);
  asm volatile(
      "s_waitcnt vmcnt(5)\n\ts_cmpk_ge_i32 %0, %1\n\ts_cbranch_scc1 1f\n\ts_waitcnt vmcnt(3)\n\t"
      "s_cmpk_ge_i32 %0, %2\n\ts_cbranch_scc1 1f\n\ts_waitcnt vmcnt(2)\n\t"
      "s_cmpk_ge_i32 %0, %3\n\ts_cbranch_scc1 1f\n\ts_waitcnt vmcnt(1)\n\t"
      "s_cmpk_ge_i32 %0, %4\n\ts_cbranch_scc1 1f\n\ts_waitcnt vmcnt(0)\n1:" ::"s"(el),
      "i"(pace_thr(N, 5)), "i"(pace_thr(N, 4)), "i"(pace_thr(N, 3)), "i"(pace_thr(N, 2))
      : "scc");
}
constexpr int issue_after(int g) {
  const int n = g < 2 * (WIN - DIST) ? ((g & 1) ? WIN + g / 2 : -1) : g + DIST;
  return n < 2 * NLD ? n : -1;
}

constexpr int L_QP = 0;
constexpr int L_P = L_QP + 8 * 64;
constexpr int L_ACC = L_P + 8 * 128;
constexpr int L_O = L_ACC + 8 * 256;
constexpr int L_ML = L_O + 128;
constexpr int L_CNT = L_ML + 16;
constexpr int L_END = L_CNT + 4;

__global__ void __launch_bounds__(NTHR, 2) mha2_decode_kernel(Params prm) {
  __shared__ __attribute__((aligned(16))) float lds[L_END];
  const unsigned t_start = (unsigned)__builtin_amdgcn_s_memrealtime();
  const int tid = threadIdx.x, lane = tid & 63;
  const int w = __builtin_amdgcn_readfirstlane(tid >> 6);
  const int bid = blockIdx.x;
  const int team = (bid & 7) * 4 + (bid >> 6);
  const int blk = team >> 4, h = team & 15;
  const int r = (bid >> 3) & 7, b = r >> 2, s = r & 3;
  const int hh = lane >> 5, l32 = lane & 31, rc = lane >> 3, c8 = lane & 7;
  const int grp = blk * 2 + b, me = s * 16 + h;
  const char* const pk = (const char*)(blk ? prm.k[1] : prm.k[0]);
  const char* const pv = (const char*)(blk ? prm.v[1] : prm.v[0]);
  const char* const pwq = (const char*)(blk ? prm.wq[1] : prm.wq[0]);
  const char* const pwo = (const char*)(blk ? prm.wo[1] : prm.wo[0]);
  const char* const px = (const char*)(blk ? prm.x[1] : prm.x[0]);

  const size_t kvoff = (size_t)(b * H + h) * SK * DH * 2;
  const rsrc_t rk = mk_rsrc(pk + kvoff, SK * DH * 2);
  const rsrc_t rv = mk_rsrc(pv + kvoff, SK * DH * 2);
  unsigned kvb = (unsigned)(grp * 16 + h) * 32768u + ((w & 1) << 9) + ((w & 6) << 11) + ((s & 1) << 14) + ((s & 2) << 15);
  unsigned kvo = 0;
  const unsigned l16 = lane * 16;
  const rsrc_t rw = mk_rsrc(prm.ws, WS_END);
  const rsrc_t rq = mk_rsrc(pwq + (size_t)(w * 56) * (H * DH * 2) + h * (DH * 2) + r * 64, 55 * (H * DH * 2) + 64);
  const rsrc_t rx = mk_rsrc(px + w * 112, DM * 2 + 112);
  const rsrc_t ro = mk_rsrc(pwo + (size_t)h * DH * (DM * 2) + w * 128, w < 7 ? 255 * (DM * 2) + 128 : 0);

  unsigned tag;
  v4u ring[WIN];
  v4u wqv[4], qg, ml;
  v2u x0, x1;
  v4u wb[32];
  v8u bt[8][2];

  auto q_share = [&]() __attribute__((always_inline)) {
    float a0[8], a1[8];
#pragma unroll
    for (int j = 0; j < 4; ++j) {
      const unsigned p0 = __builtin_amdgcn_perm(wqv[1][j], wqv[0][j], 0x05040100u), p1 = __builtin_amdgcn_perm(wqv[1][j], wqv[0][j], 0x07060302u);
      const unsigned p2 = __builtin_amdgcn_perm(wqv[3][j], wqv[2][j], 0x05040100u), p3 = __builtin_amdgcn_perm(wqv[3][j], wqv[2][j], 0x07060302u);
      a0[2 * j] = dot2(p2, x0[1], dot2(p0, x0[0], 0.f));
      a0[2 * j + 1] = dot2(p3, x0[1], dot2(p1, x0[0], 0.f));
      a1[2 * j] = dot2(p2, x1[1], dot2(p0, x1[0], 0.f));
      a1[2 * j + 1] = dot2(p3, x1[1], dot2(p1, x1[0], 0.f));
    }
    float z[8];
#pragma unroll
    for (int j = 0; j < 8; ++j) z[j] = swap32_add(a0[j], a1[j]);
    f32x4 u;
#pragma unroll
    for (int j = 0; j < 4; ++j) {
      float t = swap16_add(z[j], z[j + 4]);
      t += dppf<ROR4>(t);
      t += dppf<XOR8>(t);
      u[j] = t;
    }
    if ((lane & 12) == 0) *(f32x4*)(lds + L_QP + w * 64 + hh * 32 + (lane & 3) * 8 + ((lane >> 4) & 1) * 4) = u;
    unsigned arrived = 0;
    asm volatile("s_waitcnt lgkmcnt(0)" : "+s"(tag)::"memory");
    if (lane == 0) arrived = __hip_atomic_fetch_add((unsigned*)lds + L_CNT, 1u, __ATOMIC_RELAXED, __HIP_MEMORY_SCOPE_WORKGROUP);
    asm volatile("" ::: "memory");
    if (__builtin_amdgcn_readfirstlane(arrived) == 7) {
      float q = 0.f;
#pragma unroll
      for (int i = 0; i < 8; ++i) q += lds[L_QP + i * 64 + lane];
      const unsigned pr = pack_bf16(q, dppf<XOR1>(q));
      const unsigned pn = dppu<XOR2>(pr);
      if ((lane & 3) == 0)
        __builtin_amdgcn_raw_buffer_store_b128(v4u{pr, tag, pn, tag}, rw,
                                               WS_Q + (team * 2 + hh) * 1024 + ((lane >> 2) & 1) * 512 + (r * 4 + (l32 >> 3)) * 16, 0, AUX_SC1);
    }
    SB();
  };

  auto issue = LAMBDA(nc) {
    constexpr int n = decltype(nc)::value;
    if constexpr (n == N_WQ) {
      if (tid < 2) ((unsigned*)lds)[L_CNT + tid] = 0u;
      LDS_BARRIER();
      tag = *(const CAS unsigned*)((const CAS char*)prm.ws + WS_EPOCH + blk * 256) + 0x01010101u;
      const unsigned xoff = lane < 56 ? (lane >> 2) * 8 : OOB;
      x0 = __builtin_amdgcn_raw_buffer_load_b64(rx, xoff, 0, 0);
      x1 = __builtin_amdgcn_raw_buffer_load_b64(rx, xoff, DM * 2, 0);
#pragma unroll
      for (int i = 0; i < 4; ++i) wqv[i] = bld<0>(rq, (lane >> 2) * 4 * (H * DH * 2) + (lane & 3) * 16, i * (H * DH * 2));
    }
    if constexpr (n == N_QCALC) q_share();
    if constexpr (n >= FREE_N) throttle<n>(t_start);
    if constexpr (n == N_QPOLL) qg = bld<AUX_SC1>(rw, WS_Q + (team * 2 + b) * 1024 + l16, 0);
    if constexpr (n == N_ML) ml = bld<AUX_SC1>(rw, WS_ML + (grp * 16 + h) * 512 + l32 * 16, 0);
    SB();
    if constexpr ((n & 1) == 0) {
      asm volatile("" : "+s"(kvb));
      kvo = (kvb + ((n & 2) << 14) + ((n & 60) << 15)) & (SK * DH * 2 - 1);
    }
    ring[n % WIN] = bld<AUX_NT>(n < NLD ? rk : rv, l32 * 16 + hh * 2048 + (n & 1) * 1024, kvo);
    SB();
    static_for<0, 32>(LAMBDA(jc) {
      constexpr int j = decltype(jc)::value;
      if constexpr (n == (j < 4 ? WM0 + j * WMS : WO0 + 3 * j)) {
        wb[j] = bld<0>(ro, 32 * rc * (DM * 2) + c8 * 16 + (j & 3) * (DM * 2), (unsigned)(((j >> 2) ^ r) * 4 * (DM * 2)));
        SB();
      }
    });
    constexpr int P0 = WO0 + DIST + 1;
    static_assert(P0 + 31 * 3 < 2 * NLD, "all wo rows are paired inside the stream");
    if constexpr (n >= P0 && (n - P0) % 3 == 0 && (n - P0) / 3 < 32 && (((n - P0) / 3) & 1)) {
      constexpr int j = (n - P0) / 3, t = j >> 1;
#pragma unroll
      for (int jj = 0; jj < 4; ++jj) {
        unsigned lo = __builtin_amdgcn_perm(wb[2 * t + 1][jj], wb[2 * t][jj], 0x05040100u);
        unsigned hi = __builtin_amdgcn_perm(wb[2 * t + 1][jj], wb[2 * t][jj], 0x07060302u);
        asm volatile("" : "+v"(lo), "+v"(hi));
        bt[2 * jj][t >> 3][t & 7] = lo;
        bt[2 * jj + 1][t >> 3][t & 7] = hi;
      }
      if constexpr (j == 31) {
#pragma unroll
        for (int jj = 0; jj < 8; ++jj) asm volatile("" : "+v"(bt[jj][0]), "+v"(bt[jj][1]));
      }
      SB();
    }
  };
  auto after = LAMBDA(gc) {
    constexpr int n = issue_after(decltype(gc)::value);
    SB();
    if constexpr (n >= 0) issue(std::integral_constant<int, n>{});
  };

  static_for<0, WIN>(issue);
  if (__builtin_expect(!__all(qg[1] == tag && qg[3] == tag), 0)) {
    for (unsigned spins = 0; spins < SPIN_CAP; ++spins) {
      asm volatile("" ::: "memory");
      qg = bld<AUX_SC1>(rw, WS_Q + (team * 2 + b) * 1024 + l16, 0);
      if (__all(qg[1] == tag && qg[3] == tag)) break;
    }
  }
  v4u qv;
  {
    auto e = __builtin_amdgcn_permlane32_swap(qg[0], qg[0], false, false);
    auto o = __builtin_amdgcn_permlane32_swap(qg[2], qg[2], false, false);
    qv = v4u{e[0], o[0], e[1], o[1]};
  }

  float sc[2];
  {
    float xs[16];
    float red[2];
    static_for<0, NLD>(LAMBDA(tc) {
      constexpr int t = decltype(tc)::value;
      const v4u cur = ring[t % WIN];
      xs[t & 15] = dot2(cur[3], qv[3], dot2(cur[2], qv[2], dot2(cur[1], qv[1], dot2(cur[0], qv[0], 0.f))));
      after(tc);
      if constexpr ((t & 15) == 15) {
#define TR_STEP(HALF, CTRL)                                               \
  _Pragma("unroll") for (int u = 0; u < (HALF); ++u) {                    \
    const float lo_ = xs[u], hi_ = xs[u + (HALF)];                        \
    const float t1_ = lo_ + dppf<CTRL>(lo_), t2_ = hi_ + dppf<CTRL>(hi_); \
    xs[u] = (lane & (HALF)) ? t2_ : t1_;                                  \
  }
        TR_STEP(8, XOR8) TR_STEP(4, XOR7) TR_STEP(2, XOR2) TR_STEP(1, XOR1)
#undef TR_STEP
        asm volatile("" : "+v"(xs[0]));
        red[(t >> 4) & 1] = xs[0];
        if constexpr ((t & 31) == 31) sc[t >> 5] = swap16_add(red[0], red[1]);
        SB();
      }
    });
  }
  constexpr float C2 = 0.0625f * 1.4426950408889634f;
  const float mw = swap32_max(half_max(fmaxf(sc[0], sc[1]))) * C2;
  {
    const float p0 = __builtin_amdgcn_exp2f(fmaf(sc[0], C2, -mw));
    const float p1 = __builtin_amdgcn_exp2f(fmaf(sc[1], C2, -mw));
    lds[L_P + w * 128 + hh * 64 + l32] = p0;
    lds[L_P + w * 128 + hh * 64 + 32 + l32] = p1;
    float lw = half_sum(p0 + p1);
    lw = swap32_add(lw, lw);
    unsigned arrived = 0;
    if (lane == 0) {
      *(f32x2*)(lds + L_ML + w * 2) = f32x2{mw, lw};
      asm volatile("" ::: "memory");
      arrived = __hip_atomic_fetch_add((unsigned*)lds + L_CNT + 1, 1u, __ATOMIC_RELAXED, __HIP_MEMORY_SCOPE_WORKGROUP);
    }
    asm volatile("" ::: "memory");
    if (__builtin_amdgcn_readfirstlane(arrived) == 7) {
      const f32x2 e = *(const f32x2*)(lds + L_ML + c8 * 2);
      __builtin_amdgcn_raw_buffer_store_b128(v4u{__float_as_uint(e[0]), tag, __float_as_uint(e[1]), tag}, rw,
                                             (lane < 8 ? WS_ML : OOB) + (grp * 16 + h) * 512 + (s * 8 + c8) * 16, 0, AUX_SC1);
    }
  }
  unsigned yst = (w < 7 ? WS_Y : OOB) + ((grp * 14 + w * 2 + hh) * 64 + me) * 128 + l32 * 4;
  unsigned ytag = (tag >> ((lane & 3) * 8)) & 255u;
  unsigned accrd = L_ACC * 4 + rc * 1024 + w * 128 + c8 * 16;
  const int kk = c8 ^ r;
  unsigned owr = L_O * 4 + (((kk >> 2) * 4 + ((kk >> 1) & 1) * 2 + (w >> 2)) * 16 + hh * 8 + (w & 1) * 4 + ((w >> 1) & 1) * 2 + (kk & 1)) * 4;
  asm volatile("s_waitcnt lgkmcnt(0)" : "+v"(yst), "+v"(ytag), "+v"(accrd), "+v"(owr)::"memory");
  SB();

  f32x2 acc[4];
#pragma unroll
  for (int j = 0; j < 4; ++j) acc[j] = f32x2{0.f, 0.f};
  float fw;
  bool mlok;
  auto ml_eval = [&]() __attribute__((always_inline)) {
    mlok = __all(ml[1] == tag && ml[3] == tag);
    const float m = __uint_as_float(ml[0]);
    const float M = half_max(m);
    const float L = half_sum(__builtin_amdgcn_exp2f(m - M) * __uint_as_float(ml[2]));
    fw = __builtin_amdgcn_exp2f(mw - M) / L;
  };
  {
    const f32x4* pl = (const f32x4*)(lds + L_P + w * 128 + hh * 64);
    f32x4 pc = pl[0];
    static_for<0, NLD>(LAMBDA(tc) {
      constexpr int t = decltype(tc)::value;
      const v4u cur = ring[(t + NLD) % WIN];
      const float pv_ = pc[t & 3];
      if constexpr ((t & 3) == 3 && t + 1 < NLD) pc = pl[(t + 1) >> 2];
#pragma unroll
      for (int j = 0; j < 4; ++j) {
        const f32x2 vv = {__uint_as_float(cur[j] << 16), __uint_as_float(cur[j] & 0xffff0000u)};
        acc[j] = __builtin_elementwise_fma(vv, f32x2{pv_, pv_}, acc[j]);
      }
      asm volatile("" : "+v"(acc[0]), "+v"(acc[1]), "+v"(acc[2]), "+v"(acc[3]));
      after(std::integral_constant<int, t + NLD>{});
      if constexpr (t + NLD == G_ML) {
        ml_eval();
        asm volatile("" : "+v"(fw));
        SB();
      }
    });
  }
  if (__builtin_expect(!mlok, 0)) {
    for (unsigned spins = 0; !mlok && spins < SPIN_CAP; ++spins) {
      __builtin_amdgcn_s_sleep(8);
      asm volatile("" ::: "memory");
      ml = bld<AUX_SC1>(rw, WS_ML + (grp * 16 + h) * 512 + l32 * 16, 0);
      ml_eval();
    }
  }

  {
    f32x4 rr;
#pragma unroll
    for (int j = 0; j < 4; ++j) rr[j] = swap32_add(acc[j >> 1][j & 1], acc[2 + (j >> 1)][j & 1]) * fw;
    *(f32x4*)(lds + L_ACC + w * 256 + l32 * 8 + hh * 4) = rr;
  }
  LDS_BARRIER();
  const f32x4 a = *(const f32x4*)((const char*)lds + accrd);
  SB();
  int sidx = (lane & 2) ? (int)0xEEEEEEEEu : 0x44444444;
  unsigned ord = L_O * 4 + (lane >> 4) * 64 + (lane & 3) * 16;
  f32x4 dd[8];
#pragma unroll
  for (int j = 0; j < 8; ++j) {
    dd[j] = f32x4{0.f, 0.f, 0.f, 0.f};
    asm volatile("" : "+v"(dd[j]));
  }
  asm volatile("" : "+v"(sidx), "+v"(ord));
  SB();
  {
    float e0 = swap32_add(a[0], a[2]), e1 = swap32_add(a[1], a[3]);
    e0 += dppf<XOR8>(e0);
    e1 += dppf<XOR8>(e1);
    e0 = swap16_add(e0, e0);
    e1 = swap16_add(e1, e1);
    if ((lane & 24) == 0) *(unsigned*)((char*)lds + owr) = pack_bf16(e0, e1);
  }
  LDS_BARRIER();

  {
    const v4u oa0 = *(const v4u*)((const char*)lds + ord), oa1 = *(const v4u*)((const char*)lds + ord + 256);
#pragma unroll
    for (int j = 0; j < 8; ++j)
      dd[j] = __builtin_amdgcn_smfmac_f32_16x16x64_bf16(__builtin_bit_cast(bf16x8, oa0), __builtin_bit_cast(bf16x16, bt[j][0]), dd[j], sidx, 0, 0);
#pragma unroll
    for (int j = 0; j < 8; ++j)
      dd[j] = __builtin_amdgcn_smfmac_f32_16x16x64_bf16(__builtin_bit_cast(bf16x8, oa1), __builtin_bit_cast(bf16x16, bt[j][1]), dd[j], sidx, 0, 0);
    SB();
    float y[8];
#pragma unroll
    for (int j = 0; j < 8; ++j) y[j] = sel(lane & 8, dd[j][1] + dd[j][3], dd[j][0] + dd[j][2]);
    float u[2];
#pragma unroll
    for (int j = 0; j < 2; ++j) u[j] = sel(hh, sel(lane & 16, y[6 + j], y[4 + j]), sel(lane & 16, y[2 + j], y[j]));
    const float t0 = u[0] + dppf<XOR8>(u[0]), t1 = u[1] + dppf<XOR8>(u[1]);
    const float yv = (lane & 8) ? t1 : t0;
    __builtin_amdgcn_raw_buffer_store_b32((__float_as_uint(yv) & ~255u) | ytag, rw, yst, 0, AUX_SC1);
  }
  if (w < 7 || me >= 14) return;

  {
    const unsigned yo = WS_Y + (grp * 14 + me) * 8192 + l16;
    const v4u tg = {tag & 255u, (tag >> 8) & 255u, (tag >> 16) & 255u, tag >> 24};
    const int ls = 32 * (me & 1) + 4 * c8 + 2 * hh + ((lane >> 4) & 1);
    GAS unsigned short* optr = (GAS unsigned short*)prm.out + b * (2 * DM) + blk * DM + 64 * (me >> 1) + 8 * (ls & 7) + (ls >> 3);
    __builtin_amdgcn_s_sleep(16);
    f32x4 sum;
    for (unsigned spins = 0;; ++spins) {
      v4u g[8];
#pragma unroll
      for (int t = 0; t < 8; ++t) g[t] = bld<AUX_SC1>(rw, yo, t * 1024);
      unsigned bad = 0;
      sum = f32x4{0.f, 0.f, 0.f, 0.f};
#pragma unroll
      for (int t = 0; t < 8; ++t) {
        const v4u x = g[t] ^ tg;
        bad |= (x[0] | x[1]) | (x[2] | x[3]);
        sum += __builtin_bit_cast(f32x4, x);
      }
      if (__all((bad & 255u) == 0) || spins > SPIN_CAP) break;
      asm volatile("" ::: "memory");
    }
    float rr = swap16_add(swap32_add(sum[0], sum[2]), swap32_add(sum[1], sum[3]));
    rr += dppf<XOR8>(rr);
    if ((lane & 8) == 0) __hip_atomic_store(optr, (unsigned short)(pack_bf16(rr, rr) & 0xffffu), RLX_AGENT);
    if (me == 0 && b == 0 && lane == 0) __hip_atomic_store((GAS unsigned*)((GAS char*)prm.ws + WS_EPOCH + blk * 256), tag, RLX_AGENT);
  }
}

extern "C" void kernel_launch(void* const* d_in, const int* in_sizes, int n_in, void* d_out, int out_size, void* d_ws,
                              size_t ws_size, hipStream_t stream) {
  Params p;
  p.x[0] = d_in[0];
  p.k[0] = d_in[1];
  p.v[0] = d_in[2];
  p.wq[0] = d_in[3];
  p.wo[0] = d_in[4];
  p.x[1] = d_in[5];
  p.k[1] = d_in[6];
  p.v[1] = d_in[7];
  p.wq[1] = d_in[8];
  p.wo[1] = d_in[9];
  p.out = d_out;
  p.ws = d_ws;
  mha2_decode_kernel<<<dim3(256), dim3(NTHR), 0, stream>>>(p);
}
```

```cpp
#include <hip/hip_runtime.h>
#include <hip/hip_bf16.h>
#include <stdint.h>
#include <type_traits>

#define GAS __attribute__((address_space(1)))
#define CAS __attribute__((address_space(4)))
typedef unsigned v2u __attribute__((ext_vector_type(2)));
typedef unsigned v4u __attribute__((ext_vector_type(4)));
typedef unsigned v8u __attribute__((ext_vector_type(8)));
typedef float f32x2 __attribute__((ext_vector_type(2)));
typedef float f32x4 __attribute__((ext_vector_type(4)));
typedef __bf16 bf16x2 __attribute__((ext_vector_type(2)));
typedef __bf16 bf16x8 __attribute__((ext_vector_type(8)));
typedef __bf16 bf16x16 __attribute__((ext_vector_type(16)));
#define RLX_AGENT __ATOMIC_RELAXED, __HIP_MEMORY_SCOPE_AGENT

constexpr int WIN = 16;
constexpr int DIST = 6;
constexpr int FREE_N = 4;
constexpr int N_WQ = 2;
constexpr int N_QCALC = 6;
constexpr int N_QPOLL = 13;
constexpr int WM0 = 6, WMS = 2;
constexpr int WO0 = 12;
constexpr int N_ML = 96;
constexpr int G_ML = 123;
constexpr int PACE_T0 = 55;
constexpr int PACE_DUR = 3490;
constexpr int PACE_GAIN = 128;

constexpr int H = 16, SK = 4096, DH = 256, DM = 448;
constexpr int NTHR = 512;
constexpr int NLD = 64;
constexpr unsigned WS_EPOCH = 0;
constexpr unsigned WS_Q = 4096;
constexpr unsigned WS_ML = WS_Q + 65536;
constexpr unsigned WS_Y = WS_ML + 32768;
constexpr unsigned WS_END = WS_Y + 4 * 14 * 8192;
constexpr unsigned SPIN_CAP = 1u << 20;
constexpr unsigned OOB = 0x7fffff00u;
constexpr int AUX_NT = 2, AUX_SC1 = 16;

struct Params {
  const void* k[2];
  const void* wq[2];
  const void* x[2];
  void* ws;
  void* out;
  const void* v[2];
  const void* wo[2];
};

__device__ __forceinline__ float dot2(unsigned a, unsigned b, float c) {
  return __builtin_amdgcn_fdot2_f32_bf16(__builtin_bit_cast(bf16x2, a), __builtin_bit_cast(bf16x2, b), c, false);
}
__device__ __forceinline__ unsigned pack_bf16(float lo, float hi) {
  bf16x2 r;
  r.x = (__bf16)lo;
  r.y = (__bf16)hi;
  return __builtin_bit_cast(unsigned, r);
}
template <int CTRL> __device__ __forceinline__ float dppf(float x) {
  return __builtin_bit_cast(float, __builtin_amdgcn_mov_dpp(__builtin_bit_cast(int, x), CTRL, 0xf, 0xf, true));
}
constexpr int XOR1 = 0xB1, XOR2 = 0x4E, XOR7 = 0x141, XOR8 = 0x128, ROR4 = 0x124;
__device__ __forceinline__ float swap16_add(float a, float b) {
  auto r = __builtin_amdgcn_permlane16_swap(__float_as_uint(a), __float_as_uint(b), false, false);
  return __uint_as_float(r[0]) + __uint_as_float(r[1]);
}
__device__ __forceinline__ float swap32_add(float a, float b) {
  auto r = __builtin_amdgcn_permlane32_swap(__float_as_uint(a), __float_as_uint(b), false, false);
  return __uint_as_float(r[0]) + __uint_as_float(r[1]);
}
__device__ __forceinline__ float swap32_max(float a) {
  auto r = __builtin_amdgcn_permlane32_swap(__float_as_uint(a), __float_as_uint(a), false, false);
  return fmaxf(__uint_as_float(r[0]), __uint_as_float(r[1]));
}
__device__ __forceinline__ float half_max(float v) {
  v = fmaxf(v, dppf<XOR1>(v));
  v = fmaxf(v, dppf<XOR2>(v));
  v = fmaxf(v, dppf<XOR7>(v));
  v = fmaxf(v, dppf<XOR8>(v));
  auto r = __builtin_amdgcn_permlane16_swap(__float_as_uint(v), __float_as_uint(v), false, false);
  return fmaxf(__uint_as_float(r[0]), __uint_as_float(r[1]));
}
__device__ __forceinline__ float half_sum(float v) {
  v += dppf<XOR1>(v);
  v += dppf<XOR2>(v);
  v += dppf<XOR7>(v);
  v += dppf<XOR8>(v);
  return swap16_add(v, v);
}
__device__ __forceinline__ float sel(bool c, float a, float b) {
  asm volatile("" : "+v"(a), "+v"(b));
  return c ? a : b;
}
__device__ __forceinline__ bool tagged16(v4u g, unsigned tag) {
  return __builtin_amdgcn_perm(g[1], g[0], 0x07060302u) == tag && __builtin_amdgcn_perm(g[3], g[2], 0x07060302u) == tag;
}
template <int I, int N, class F> __device__ __forceinline__ void static_for(F&& f) {
  if constexpr (I < N) {
    f(std::integral_constant<int, I>{});
    static_for<I + 1, N>(f);
  }
}
#define LAMBDA(v) [&](auto v) __attribute__((always_inline))
typedef __amdgpu_buffer_rsrc_t rsrc_t;
__device__ __forceinline__ rsrc_t mk_rsrc(const void* base, unsigned bytes) {
  return __builtin_amdgcn_make_buffer_rsrc((void*)base, 0, bytes, 0x00020000);
}
template <int AUX> __device__ __forceinline__ v4u bld(rsrc_t rs, unsigned voff, unsigned soff) {
  return __builtin_amdgcn_raw_buffer_load_b128(rs, voff, soff, AUX);
}
#define SB() __builtin_amdgcn_sched_barrier(0)
#define LDS_BARRIER()                                  \
  do {                                                 \
    asm volatile("s_waitcnt lgkmcnt(0)" ::: "memory"); \
    __builtin_amdgcn_s_barrier();                      \
    asm volatile("" ::: "memory");                     \
    SB();                                              \
  } while (0)

constexpr int pace_thr(int n, int depth) {
  const int dith16 = 2 * (((n & 1) << 2) | (n & 2) | ((n & 4) >> 2)) + 1;
  const int lag = depth == 2   ? -50
                  : depth <= 4 ? PACE_GAIN * (16 * (depth - 2) - dith16) / 16
                               : 2 * PACE_GAIN + (PACE_GAIN + 300 * n / 128) * (16 - dith16) / 16;
  return PACE_T0 + n * PACE_DUR / 128 + lag;
}
template <int N> __device__ __forceinline__ void throttle(unsigned t_start) {
  const int el = (int)((unsigned)__builtin_amdgcn_s_memrealtime() - t_start);
  asm volatile(
      "s_waitcnt vmcnt(5)\n\ts_cmpk_ge_i32 %0, %1\n\ts_cbranch_scc1 1f\n\ts_waitcnt vmcnt(3)\n\t"
      "s_cmpk_ge_i32 %0, %2\n\ts_cbranch_scc1 1f\n\ts_waitcnt vmcnt(2)\n\t"
      "s_cmpk_ge_i32 %0, %3\n\ts_cbranch_scc1 1f\n\ts_waitcnt vmcnt(1)\n\t"
      "s_cmpk_ge_i32 %0, %4\n\ts_cbranch_scc1 1f\n\ts_waitcnt vmcnt(0)\n1:" ::"s"(el),
      "i"(pace_thr(N, 5)), "i"(pace_thr(N, 4)), "i"(pace_thr(N, 3)), "i"(pace_thr(N, 2))
      : "scc");
}
constexpr int issue_after(int g) {
  const int n = g < 2 * (WIN - DIST) ? ((g & 1) ? WIN + g / 2 : -1) : g + DIST;
  return n < 2 * NLD ? n : -1;
}

constexpr int L_QP = 0;
constexpr int L_P = L_QP + 8 * 64;
constexpr int L_ACC = L_P + 8 * 128;
constexpr int L_O = L_ACC + 8 * 256;
constexpr int L_ML = L_O + 128;
constexpr int L_CNT = L_ML + 16;
constexpr int L_END = L_CNT + 4;

__global__ void __launch_bounds__(NTHR, 2) mha2_decode_kernel(Params prm) {
  __shared__ __attribute__((aligned(16))) float lds[L_END];
  const unsigned t_start = (unsigned)__builtin_amdgcn_s_memrealtime();
  const int tid = threadIdx.x, lane = tid & 63;
  const int w = __builtin_amdgcn_readfirstlane(tid >> 6);
  const int bid = blockIdx.x;
  const int team = (bid & 7) * 4 + (bid >> 6);
  const int blk = team >> 4, h = team & 15;
  const int r = (bid >> 3) & 7, b = r >> 2, s = r & 3;
  const int hh = lane >> 5, l32 = lane & 31, rc = lane >> 3, c8 = lane & 7;
  const int grp = blk * 2 + b, me = s * 16 + h;
  const char* const pk = (const char*)(blk ? prm.k[1] : prm.k[0]);
  const char* const pv = (const char*)(blk ? prm.v[1] : prm.v[0]);
  const char* const pwq = (const char*)(blk ? prm.wq[1] : prm.wq[0]);
  const char* const pwo = (const char*)(blk ? prm.wo[1] : prm.wo[0]);
  const char* const px = (const char*)(blk ? prm.x[1] : prm.x[0]);

  const size_t kvoff = (size_t)(b * H + h) * SK * DH * 2;
  const rsrc_t rk = mk_rsrc(pk + kvoff, SK * DH * 2);
  const rsrc_t rv = mk_rsrc(pv + kvoff, SK * DH * 2);
  unsigned kvb = (unsigned)(grp * 16 + h) * 32768u + ((w & 1) << 9) + ((w & 6) << 11) + ((s & 1) << 14) + ((s & 2) << 15);
  unsigned kvo = 0;
  const unsigned l16 = lane * 16;
  const rsrc_t rw = mk_rsrc(prm.ws, WS_END);
  const rsrc_t rq = mk_rsrc(pwq + (size_t)(w * 56) * (H * DH * 2) + h * (DH * 2) + r * 64, 55 * (H * DH * 2) + 64);
  const rsrc_t rx = mk_rsrc(px + w * 112, DM * 2 + 112);
  const rsrc_t ro = mk_rsrc(pwo + (size_t)h * DH * (DM * 2) + w * 128, w < 7 ? 255 * (DM * 2) + 128 : 0);

  unsigned tag;
  v4u ring[WIN];
  v4u wqv[4], qg, ml;
  v2u x0, x1;
  v4u wb[32];
  v8u bt[8][2];

  auto q_share = [&]() __attribute__((always_inline)) {
    float a0[8], a1[8];
#pragma unroll
    for (int j = 0; j < 4; ++j) {
      const unsigned p0 = __builtin_amdgcn_perm(wqv[1][j], wqv[0][j], 0x05040100u), p1 = __builtin_amdgcn_perm(wqv[1][j], wqv[0][j], 0x07060302u);
      const unsigned p2 = __builtin_amdgcn_perm(wqv[3][j], wqv[2][j], 0x05040100u), p3 = __builtin_amdgcn_perm(wqv[3][j], wqv[2][j], 0x07060302u);
      a0[2 * j] = dot2(p2, x0[1], dot2(p0, x0[0], 0.f));
      a0[2 * j + 1] = dot2(p3, x0[1], dot2(p1, x0[0], 0.f));
      a1[2 * j] = dot2(p2, x1[1], dot2(p0, x1[0], 0.f));
      a1[2 * j + 1] = dot2(p3, x1[1], dot2(p1, x1[0], 0.f));
    }
    float z[8];
#pragma unroll
    for (int j = 0; j < 8; ++j) z[j] = swap32_add(a0[j], a1[j]);
    f32x4 u;
#pragma unroll
    for (int j = 0; j < 4; ++j) {
      float t = swap16_add(z[j], z[j + 4]);
      t += dppf<ROR4>(t);
      t += dppf<XOR8>(t);
      u[j] = t;
    }
    if ((lane & 12) == 0) *(f32x4*)(lds + L_QP + w * 64 + hh * 32 + (lane & 3) * 8 + ((lane >> 4) & 1) * 4) = u;
    unsigned arrived = 0;
    asm volatile("s_waitcnt lgkmcnt(0)" : "+s"(tag)::"memory");
    if (lane == 0) arrived = __hip_atomic_fetch_add((unsigned*)lds + L_CNT, 1u, __ATOMIC_RELAXED, __HIP_MEMORY_SCOPE_WORKGROUP);
    asm volatile("" ::: "memory");
    if (__builtin_amdgcn_readfirstlane(arrived) == 7) {
      float q = 0.f;
#pragma unroll
      for (int i = 0; i < 8; ++i) q += lds[L_QP + i * 64 + lane];
      __builtin_amdgcn_raw_buffer_store_b32((pack_bf16(q, q) & 0xffffu) | (((lane & 1) ? tag : tag << 16) & 0xffff0000u), rw,
                                            WS_Q + (team * 2 + hh) * 1024 + ((lane >> 2) & 1) * 512 + (r * 4 + (l32 >> 3)) * 16 + (lane & 3) * 4, 0, AUX_SC1);
    }
    SB();
  };

  auto issue = LAMBDA(nc) {
    constexpr int n = decltype(nc)::value;
    if constexpr (n == N_WQ) {
      if (tid < 2) ((unsigned*)lds)[L_CNT + tid] = 0u;
      LDS_BARRIER();
      tag = *(const CAS unsigned*)((const CAS char*)prm.ws + WS_EPOCH + blk * 256) + 0x01010101u;
      const unsigned xoff = lane < 56 ? (lane >> 2) * 8 : OOB;
      x0 = __builtin_amdgcn_raw_buffer_load_b64(rx, xoff, 0, 0);
      x1 = __builtin_amdgcn_raw_buffer_load_b64(rx, xoff, DM * 2, 0);
#pragma unroll
      for (int i = 0; i < 4; ++i) wqv[i] = bld<0>(rq, (lane >> 2) * 4 * (H * DH * 2) + (lane & 3) * 16, i * (H * DH * 2));
    }
    if constexpr (n == N_QCALC) q_share();
    if constexpr (n >= FREE_N) throttle<n>(t_start);
    if constexpr (n == N_QPOLL) qg = bld<AUX_SC1>(rw, WS_Q + (team * 2 + b) * 1024 + l16, 0);
    if constexpr (n == N_ML) ml = bld<AUX_SC1>(rw, WS_ML + (grp * 16 + h) * 512 + l32 * 16, 0);
    SB();
    if constexpr ((n & 1) == 0) {
      asm volatile("" : "+s"(kvb));
      kvo = (kvb + ((n & 2) << 14) + ((n & 60) << 15)) & (SK * DH * 2 - 1);
    }
    ring[n % WIN] = bld<AUX_NT>(n < NLD ? rk : rv, l32 * 16 + hh * 2048 + (n & 1) * 1024, kvo);
    SB();
    static_for<0, 32>(LAMBDA(jc) {
      constexpr int j = decltype(jc)::value;
      if constexpr (n == (j < 4 ? WM0 + j * WMS : WO0 + 3 * j)) {
        wb[j] = bld<0>(ro, 32 * rc * (DM * 2) + c8 * 16 + (j & 3) * (DM * 2), (unsigned)(((j >> 2) ^ r) * 4 * (DM * 2)));
        SB();
      }
    });
    constexpr int P0 = WO0 + DIST + 1;
    static_assert(P0 + 31 * 3 < 2 * NLD, "all wo rows are paired inside the stream");
    if constexpr (n >= P0 && (n - P0) % 3 == 0 && (n - P0) / 3 < 32 && (((n - P0) / 3) & 1)) {
      constexpr int j = (n - P0) / 3, t = j >> 1;
#pragma unroll
      for (int jj = 0; jj < 4; ++jj) {
        unsigned lo = __builtin_amdgcn_perm(wb[2 * t + 1][jj], wb[2 * t][jj], 0x05040100u);
        unsigned hi = __builtin_amdgcn_perm(wb[2 * t + 1][jj], wb[2 * t][jj], 0x07060302u);
        asm volatile("" : "+v"(lo), "+v"(hi));
        bt[2 * jj][t >> 3][t & 7] = lo;
        bt[2 * jj + 1][t >> 3][t & 7] = hi;
      }
      if constexpr (j == 31) {
#pragma unroll
        for (int jj = 0; jj < 8; ++jj) asm volatile("" : "+v"(bt[jj][0]), "+v"(bt[jj][1]));
      }
      SB();
    }
  };
  auto after = LAMBDA(gc) {
    constexpr int n = issue_after(decltype(gc)::value);
    SB();
    if constexpr (n >= 0) issue(std::integral_constant<int, n>{});
  };

  static_for<0, WIN>(issue);
  if (__builtin_expect(!__all(tagged16(qg, tag)), 0)) {
    for (unsigned spins = 0; spins < SPIN_CAP; ++spins) {
      asm volatile("" ::: "memory");
      qg = bld<AUX_SC1>(rw, WS_Q + (team * 2 + b) * 1024 + l16, 0);
      if (__all(tagged16(qg, tag))) break;
    }
  }
  v4u qv;
  {
    const unsigned q0 = __builtin_amdgcn_perm(qg[1], qg[0], 0x05040100u), q1 = __builtin_amdgcn_perm(qg[3], qg[2], 0x05040100u);
    auto e = __builtin_amdgcn_permlane32_swap(q0, q0, false, false);
    auto o = __builtin_amdgcn_permlane32_swap(q1, q1, false, false);
    qv = v4u{e[0], o[0], e[1], o[1]};
  }

  float sc[2];
  {
    float xs[16];
    float red[2];
    static_for<0, NLD>(LAMBDA(tc) {
      constexpr int t = decltype(tc)::value;
      const v4u cur = ring[t % WIN];
      xs[t & 15] = dot2(cur[3], qv[3], dot2(cur[2], qv[2], dot2(cur[1], qv[1], dot2(cur[0], qv[0], 0.f))));
      after(tc);
      if constexpr ((t & 15) == 15) {
#define TR_STEP(HALF, CTRL)                                               \
  _Pragma("unroll") for (int u = 0; u < (HALF); ++u) {                    \
    const float lo_ = xs[u], hi_ = xs[u + (HALF)];                        \
    const float t1_ = lo_ + dppf<CTRL>(lo_), t2_ = hi_ + dppf<CTRL>(hi_); \
    xs[u] = (lane & (HALF)) ? t2_ : t1_;                                  \
  }
        TR_STEP(8, XOR8) TR_STEP(4, XOR7) TR_STEP(2, XOR2) TR_STEP(1, XOR1)
#undef TR_STEP
        asm volatile("" : "+v"(xs[0]));
        red[(t >> 4) & 1] = xs[0];
        if constexpr ((t & 31) == 31) sc[t >> 5] = swap16_add(red[0], red[1]);
        SB();
      }
    });
  }
  constexpr float C2 = 0.0625f * 1.4426950408889634f;
  const float mw = swap32_max(half_max(fmaxf(sc[0], sc[1]))) * C2;
  {
    const float p0 = __builtin_amdgcn_exp2f(fmaf(sc[0], C2, -mw));
    const float p1 = __builtin_amdgcn_exp2f(fmaf(sc[1], C2, -mw));
    lds[L_P + w * 128 + hh * 64 + l32] = p0;
    lds[L_P + w * 128 + hh * 64 + 32 + l32] = p1;
    float lw = half_sum(p0 + p1);
    lw = swap32_add(lw, lw);
    unsigned arrived = 0;
    if (lane == 0) {
      *(f32x2*)(lds + L_ML + w * 2) = f32x2{mw, lw};
      asm volatile("" ::: "memory");
      arrived = __hip_atomic_fetch_add((unsigned*)lds + L_CNT + 1, 1u, __ATOMIC_RELAXED, __HIP_MEMORY_SCOPE_WORKGROUP);
    }
    asm volatile("" ::: "memory");
    if (__builtin_amdgcn_readfirstlane(arrived) == 7) {
      const v2u e = *(const v2u*)(lds + L_ML + c8 * 2);
      __builtin_amdgcn_raw_buffer_store_b128(v4u{__builtin_amdgcn_perm(tag, e[0], 0x05040100u), __builtin_amdgcn_perm(tag, e[0], 0x07060302u),
                                                 __builtin_amdgcn_perm(tag, e[1], 0x05040100u), __builtin_amdgcn_perm(tag, e[1], 0x07060302u)}, rw,
                                             (lane < 8 ? WS_ML : OOB) + (grp * 16 + h) * 512 + (s * 8 + c8) * 16, 0, AUX_SC1);
    }
  }
  unsigned yst = (w < 7 ? WS_Y : OOB) + ((grp * 14 + w * 2 + hh) * 64 + me) * 128 + l32 * 4;
  unsigned ytag = (tag >> ((lane & 3) * 8)) & 255u;
  unsigned accrd = L_ACC * 4 + rc * 1024 + w * 128 + c8 * 16;
  const int kk = c8 ^ r;
  unsigned owr = L_O * 4 + (((kk >> 2) * 4 + ((kk >> 1) & 1) * 2 + (w >> 2)) * 16 + hh * 8 + (w & 1) * 4 + ((w >> 1) & 1) * 2 + (kk & 1)) * 4;
  asm volatile("s_waitcnt lgkmcnt(0)" : "+v"(yst), "+v"(ytag), "+v"(accrd), "+v"(owr)::"memory");
  SB();

  f32x2 acc[4];
#pragma unroll
  for (int j = 0; j < 4; ++j) acc[j] = f32x2{0.f, 0.f};
  float fw;
  bool mlok;
  auto ml_eval = [&]() __attribute__((always_inline)) {
    mlok = __all(tagged16(ml, tag));
    const float m = __uint_as_float(__builtin_amdgcn_perm(ml[1], ml[0], 0x05040100u));
    const float M = half_max(m);
    const float L = half_sum(__builtin_amdgcn_exp2f(m - M) * __uint_as_float(__builtin_amdgcn_perm(ml[3], ml[2], 0x05040100u)));
    fw = __builtin_amdgcn_exp2f(mw - M) / L;
  };
  {
    const f32x4* pl = (const f32x4*)(lds + L_P + w * 128 + hh * 64);
    f32x4 pc = pl[0];
    static_for<0, NLD>(LAMBDA(tc) {
      constexpr int t = decltype(tc)::value;
      const v4u cur = ring[(t + NLD) % WIN];
      const float pv_ = pc[t & 3];
      if constexpr ((t & 3) == 3 && t + 1 < NLD) pc = pl[(t + 1) >> 2];
#pragma unroll
      for (int j = 0; j < 4; ++j) {
        const f32x2 vv = {__uint_as_float(cur[j] << 16), __uint_as_float(cur[j] & 0xffff0000u)};
        acc[j] = __builtin_elementwise_fma(vv, f32x2{pv_, pv_}, acc[j]);
      }
      asm volatile("" : "+v"(acc[0]), "+v"(acc[1]), "+v"(acc[2]), "+v"(acc[3]));
      after(std::integral_constant<int, t + NLD>{});
      if constexpr (t + NLD == G_ML) {
        ml_eval();
        asm volatile("" : "+v"(fw));
        SB();
      }
    });
  }
  if (__builtin_expect(!mlok, 0)) {
    for (unsigned spins = 0; !mlok && spins < SPIN_CAP; ++spins) {
      __builtin_amdgcn_s_sleep(8);
      asm volatile("" ::: "memory");
      ml = bld<AUX_SC1>(rw, WS_ML + (grp * 16 + h) * 512 + l32 * 16, 0);
      ml_eval();
    }
  }

  {
    f32x4 rr;
#pragma unroll
    for (int j = 0; j < 4; ++j) rr[j] = swap32_add(acc[j >> 1][j & 1], acc[2 + (j >> 1)][j & 1]) * fw;
    *(f32x4*)(lds + L_ACC + w * 256 + l32 * 8 + hh * 4) = rr;
  }
  LDS_BARRIER();
  const f32x4 a = *(const f32x4*)((const char*)lds + accrd);
  SB();
  int sidx = (lane & 2) ? (int)0xEEEEEEEEu : 0x44444444;
  unsigned ord = L_O * 4 + (lane >> 4) * 64 + (lane & 3) * 16;
  f32x4 dd[8];
#pragma unroll
  for (int j = 0; j < 8; ++j) {
    dd[j] = f32x4{0.f, 0.f, 0.f, 0.f};
    asm volatile("" : "+v"(dd[j]));
  }
  asm volatile("" : "+v"(sidx), "+v"(ord));
  SB();
  {
    float e0 = swap32_add(a[0], a[2]), e1 = swap32_add(a[1], a[3]);
    e0 += dppf<XOR8>(e0);
    e1 += dppf<XOR8>(e1);
    e0 = swap16_add(e0, e0);
    e1 = swap16_add(e1, e1);
    if ((lane & 24) == 0) *(unsigned*)((char*)lds + owr) = pack_bf16(e0, e1);
  }
  LDS_BARRIER();

  {
    const v4u oa0 = *(const v4u*)((const char*)lds + ord), oa1 = *(const v4u*)((const char*)lds + ord + 256);
#pragma unroll
    for (int j = 0; j < 8; ++j)
      dd[j] = __builtin_amdgcn_smfmac_f32_16x16x64_bf16(__builtin_bit_cast(bf16x8, oa0), __builtin_bit_cast(bf16x16, bt[j][0]), dd[j], sidx, 0, 0);
#pragma unroll
    for (int j = 0; j < 8; ++j)
      dd[j] = __builtin_amdgcn_smfmac_f32_16x16x64_bf16(__builtin_bit_cast(bf16x8, oa1), __builtin_bit_cast(bf16x16, bt[j][1]), dd[j], sidx, 0, 0);
    SB();
    float y[8];
#pragma unroll
    for (int j = 0; j < 8; ++j) y[j] = sel(lane & 8, dd[j][1] + dd[j][3], dd[j][0] + dd[j][2]);
    float u[2];
#pragma unroll
    for (int j = 0; j < 2; ++j) u[j] = sel(hh, sel(lane & 16, y[6 + j], y[4 + j]), sel(lane & 16, y[2 + j], y[j]));
    const float t0 = u[0] + dppf<XOR8>(u[0]), t1 = u[1] + dppf<XOR8>(u[1]);
    const float yv = (lane & 8) ? t1 : t0;
    __builtin_amdgcn_raw_buffer_store_b32((__float_as_uint(yv) & ~255u) | ytag, rw, yst, 0, AUX_SC1);
  }
  if (w < 7 || me >= 14) return;

  {
    const unsigned yo = WS_Y + (grp * 14 + me) * 8192 + l16;
    const v4u tg = {tag & 255u, (tag >> 8) & 255u, (tag >> 16) & 255u, tag >> 24};
    const int ls = 32 * (me & 1) + 4 * c8 + 2 * hh + ((lane >> 4) & 1);
    GAS unsigned short* optr = (GAS unsigned short*)prm.out + b * (2 * DM) + blk * DM + 64 * (me >> 1) + 8 * (ls & 7) + (ls >> 3);
    __builtin_amdgcn_s_sleep(16);
    f32x4 sum;
    for (unsigned spins = 0;; ++spins) {
      v4u g[8];
#pragma unroll
      for (int t = 0; t < 8; ++t) g[t] = bld<AUX_SC1>(rw, yo, t * 1024);
      unsigned bad = 0;
      sum = f32x4{0.f, 0.f, 0.f, 0.f};
#pragma unroll
      for (int t = 0; t < 8; ++t) {
        const v4u x = g[t] ^ tg;
        bad |= (x[0] | x[1]) | (x[2] | x[3]);
        sum += __builtin_bit_cast(f32x4, x);
      }
      if (__all((bad & 255u) == 0) || spins > SPIN_CAP) break;
      asm volatile("" ::: "memory");
    }
    float rr = swap16_add(swap32_add(sum[0], sum[2]), swap32_add(sum[1], sum[3]));
    rr += dppf<XOR8>(rr);
    if ((lane & 8) == 0) __hip_atomic_store(optr, (unsigned short)(pack_bf16(rr, rr) & 0xffffu), RLX_AGENT);
    if (me == 0 && b == 0 && lane == 0) __hip_atomic_store((GAS unsigned*)((GAS char*)prm.ws + WS_EPOCH + blk * 256), tag, RLX_AGENT);
  }
}

extern "C" void kernel_launch(void* const* d_in, const int* in_sizes, int n_in, void* d_out, int out_size, void* d_ws,
                              size_t ws_size, hipStream_t stream) {
  Params p;
  p.x[0] = d_in[0];
  p.k[0] = d_in[1];
  p.v[0] = d_in[2];
  p.wq[0] = d_in[3];
  p.wo[0] = d_in[4];
  p.x[1] = d_in[5];
  p.k[1] = d_in[6];
  p.v[1] = d_in[7];
  p.wq[1] = d_in[8];
  p.wo[1] = d_in[9];
  p.out = d_out;
  p.ws = d_ws;
  mha2_decode_kernel<<<dim3(256), dim3(NTHR), 0, stream>>>(p);
}
```

```cpp
#define P_TN 20
#define P_TOFF 5
#define SK_MODE 1
#include <hip/hip_runtime.h>
#include <hip/hip_bf16.h>
#include <stdint.h>
#include <type_traits>

#define GAS __attribute__((address_space(1)))
#define CAS __attribute__((address_space(4)))
typedef unsigned v2u __attribute__((ext_vector_type(2)));
typedef unsigned v4u __attribute__((ext_vector_type(4)));
typedef unsigned v8u __attribute__((ext_vector_type(8)));
typedef float f32x2 __attribute__((ext_vector_type(2)));
typedef float f32x4 __attribute__((ext_vector_type(4)));
typedef __bf16 bf16x2 __attribute__((ext_vector_type(2)));
typedef __bf16 bf16x8 __attribute__((ext_vector_type(8)));
typedef __bf16 bf16x16 __attribute__((ext_vector_type(16)));
#define RLX_AGENT __ATOMIC_RELAXED, __HIP_MEMORY_SCOPE_AGENT

constexpr int WIN = 16;
constexpr int DIST = 6;
constexpr int FREE_N = 4;
constexpr int N_WQ = 2;
constexpr int N_QCALC = 6;
constexpr int N_QPOLL = 13;
constexpr int WM0 = 6, WMS = 2;
constexpr int WO0 = 12;
constexpr int N_ML = 96;
constexpr int G_ML = 123;
constexpr int PACE_T0 = 55;
constexpr int PACE_DUR = 3490;
constexpr int PACE_GAIN = 128;

constexpr int H = 16, SK = 4096, DH = 256, DM = 448;
constexpr int NTHR = 512;
constexpr int NLD = 64;
constexpr unsigned WS_EPOCH = 0;
constexpr unsigned WS_Q = 4096;
constexpr unsigned WS_ML = WS_Q + 65536;
constexpr unsigned WS_Y = WS_ML + 32768;
constexpr unsigned WS_T = WS_Y + 4 * 14 * 8192;
constexpr unsigned WS_END = WS_T + 64 * 128;
constexpr unsigned SPIN_CAP = 1u << 20;
constexpr unsigned OOB = 0x7fffff00u;
constexpr int AUX_NT = 2, AUX_SC1 = 16;

struct Params {
  const void* k[2];
  const void* wq[2];
  const void* x[2];
  void* ws;
  void* out;
  const void* v[2];
  const void* wo[2];
};

__device__ __forceinline__ float dot2(unsigned a, unsigned b, float c) {
  return __builtin_amdgcn_fdot2_f32_bf16(__builtin_bit_cast(bf16x2, a), __builtin_bit_cast(bf16x2, b), c, false);
}
__device__ __forceinline__ unsigned pack_bf16(float lo, float hi) {
  bf16x2 r;
  r.x = (__bf16)lo;
  r.y = (__bf16)hi;
  return __builtin_bit_cast(unsigned, r);
}
template <int CTRL> __device__ __forceinline__ float dppf(float x) {
  return __builtin_bit_cast(float, __builtin_amdgcn_mov_dpp(__builtin_bit_cast(int, x), CTRL, 0xf, 0xf, true));
}
constexpr int XOR1 = 0xB1, XOR2 = 0x4E, XOR7 = 0x141, XOR8 = 0x128, ROR4 = 0x124;
__device__ __forceinline__ float swap16_add(float a, float b) {
  auto r = __builtin_amdgcn_permlane16_swap(__float_as_uint(a), __float_as_uint(b), false, false);
  return __uint_as_float(r[0]) + __uint_as_float(r[1]);
}
__device__ __forceinline__ float swap32_add(float a, float b) {
  auto r = __builtin_amdgcn_permlane32_swap(__float_as_uint(a), __float_as_uint(b), false, false);
  return __uint_as_float(r[0]) + __uint_as_float(r[1]);
}
__device__ __forceinline__ float swap32_max(float a) {
  auto r = __builtin_amdgcn_permlane32_swap(__float_as_uint(a), __float_as_uint(a), false, false);
  return fmaxf(__uint_as_float(r[0]), __uint_as_float(r[1]));
}
__device__ __forceinline__ float half_max(float v) {
  v = fmaxf(v, dppf<XOR1>(v));
  v = fmaxf(v, dppf<XOR2>(v));
  v = fmaxf(v, dppf<XOR7>(v));
  v = fmaxf(v, dppf<XOR8>(v));
  auto r = __builtin_amdgcn_permlane16_swap(__float_as_uint(v), __float_as_uint(v), false, false);
  return fmaxf(__uint_as_float(r[0]), __uint_as_float(r[1]));
}
__device__ __forceinline__ float half_sum(float v) {
  v += dppf<XOR1>(v);
  v += dppf<XOR2>(v);
  v += dppf<XOR7>(v);
  v += dppf<XOR8>(v);
  return swap16_add(v, v);
}
__device__ __forceinline__ float sel(bool c, float a, float b) {
  asm volatile("" : "+v"(a), "+v"(b));
  return c ? a : b;
}
__device__ __forceinline__ bool tagged16(v4u g, unsigned tag) {
  return __builtin_amdgcn_perm(g[1], g[0], 0x07060302u) == tag && __builtin_amdgcn_perm(g[3], g[2], 0x07060302u) == tag;
}
template <int I, int N, class F> __device__ __forceinline__ void static_for(F&& f) {
  if constexpr (I < N) {
    f(std::integral_constant<int, I>{});
    static_for<I + 1, N>(f);
  }
}
#define LAMBDA(v) [&](auto v) __attribute__((always_inline))
typedef __amdgpu_buffer_rsrc_t rsrc_t;
__device__ __forceinline__ rsrc_t mk_rsrc(const void* base, unsigned bytes) {
  return __builtin_amdgcn_make_buffer_rsrc((void*)base, 0, bytes, 0x00020000);
}
template <int AUX> __device__ __forceinline__ v4u bld(rsrc_t rs, unsigned voff, unsigned soff) {
  return __builtin_amdgcn_raw_buffer_load_b128(rs, voff, soff, AUX);
}
#define SB() __builtin_amdgcn_sched_barrier(0)
#define LDS_BARRIER()                                  \
  do {                                                 \
    asm volatile("s_waitcnt lgkmcnt(0)" ::: "memory"); \
    __builtin_amdgcn_s_barrier();                      \
    asm volatile("" ::: "memory");                     \
    SB();                                              \
  } while (0)

constexpr int pace_thr(int n, int depth) {
  const int dith16 = 2 * (((n & 1) << 2) | (n & 2) | ((n & 4) >> 2)) + 1;
  const int lag = depth == 2   ? -50
                  : depth <= 4 ? PACE_GAIN * (16 * (depth - 2) - dith16) / 16
                               : 2 * PACE_GAIN + (PACE_GAIN + 300 * n / 128) * (16 - dith16) / 16;
  return PACE_T0 + n * PACE_DUR / 128 + lag;
}
template <int N> __device__ __forceinline__ void throttle(unsigned t_start) {
  const int el = (int)((unsigned)__builtin_amdgcn_s_memrealtime() - t_start);
  asm volatile(
      "s_waitcnt vmcnt(5)\n\ts_cmpk_ge_i32 %0, %1\n\ts_cbranch_scc1 1f\n\ts_waitcnt vmcnt(3)\n\t"
      "s_cmpk_ge_i32 %0, %2\n\ts_cbranch_scc1 1f\n\ts_waitcnt vmcnt(2)\n\t"
      "s_cmpk_ge_i32 %0, %3\n\ts_cbranch_scc1 1f\n\ts_waitcnt vmcnt(1)\n\t"
      "s_cmpk_ge_i32 %0, %4\n\ts_cbranch_scc1 1f\n\ts_waitcnt vmcnt(0)\n1:" ::"s"(el),
      "i"(pace_thr(N, 5)), "i"(pace_thr(N, 4)), "i"(pace_thr(N, 3)), "i"(pace_thr(N, 2))
      : "scc");
}
constexpr int issue_after(int g) {
  const int n = g < 2 * (WIN - DIST) ? ((g & 1) ? WIN + g / 2 : -1) : g + DIST;
  return n < 2 * NLD ? n : -1;
}

#define ST(k)
#define ST_FLUSH()

constexpr int L_QP = 0;
constexpr int L_P = L_QP + 8 * 64;
constexpr int L_ACC = L_P + 8 * 128;
constexpr int L_O = L_ACC + 8 * 256;
constexpr int L_ML = L_O + 128;
constexpr int L_CNT = L_ML + 16;
constexpr int L_END = L_CNT + 4;

__global__ void __launch_bounds__(NTHR, 2) mha2_decode_kernel(Params prm) {
  __shared__ __attribute__((aligned(16))) float lds[L_END];
  ST(0);
  unsigned t_start = (unsigned)__builtin_amdgcn_s_memrealtime();
  const int tid = threadIdx.x, lane = tid & 63;
  const int w = __builtin_amdgcn_readfirstlane(tid >> 6);
  const int bid = blockIdx.x;
  const int team = (bid & 7) * 4 + (bid >> 6);
  const int blk = team >> 4, h = team & 15;
  const int r = (bid >> 3) & 7, b = r >> 2, s = r & 3;
  const int hh = lane >> 5, l32 = lane & 31, rc = lane >> 3, c8 = lane & 7;
  const int grp = blk * 2 + b, me = s * 16 + h;
  const char* const pk = (const char*)(blk ? prm.k[1] : prm.k[0]);
  const char* const pv = (const char*)(blk ? prm.v[1] : prm.v[0]);
  const char* const pwq = (const char*)(blk ? prm.wq[1] : prm.wq[0]);
  const char* const pwo = (const char*)(blk ? prm.wo[1] : prm.wo[0]);
  const char* const px = (const char*)(blk ? prm.x[1] : prm.x[0]);

  const size_t kvoff = (size_t)(b * H + h) * SK * DH * 2;
  const rsrc_t rk = mk_rsrc(pk + kvoff, SK * DH * 2);
  const rsrc_t rv = mk_rsrc(pv + kvoff, SK * DH * 2);
  unsigned kvb = (unsigned)(grp * 16 + h) * 32768u + ((w & 1) << 9) + ((w & 6) << 11) + ((s & 1) << 14) + ((s & 2) << 15);
  unsigned kvo = 0;
  const unsigned l16 = lane * 16;
  const rsrc_t rw = mk_rsrc(prm.ws, WS_END);
  const rsrc_t rq = mk_rsrc(pwq + (size_t)(w * 56) * (H * DH * 2) + h * (DH * 2) + r * 64, 55 * (H * DH * 2) + 64);
  const rsrc_t rx = mk_rsrc(px + w * 112, DM * 2 + 112);
  const rsrc_t ro = mk_rsrc(pwo + (size_t)h * DH * (DM * 2) + w * 128, w < 7 ? 255 * (DM * 2) + 128 : 0);

  unsigned tag;
  v4u ring[WIN];
  v4u wqv[4], qg, ml;
  v2u x0, x1;
  unsigned tv;
  v4u wb[32];
  v8u bt[8][2];

  auto q_share = [&]() __attribute__((always_inline)) {
    float a0[8], a1[8];
#pragma unroll
    for (int j = 0; j < 4; ++j) {
      const unsigned p0 = __builtin_amdgcn_perm(wqv[1][j], wqv[0][j], 0x05040100u), p1 = __builtin_amdgcn_perm(wqv[1][j], wqv[0][j], 0x07060302u);
      const unsigned p2 = __builtin_amdgcn_perm(wqv[3][j], wqv[2][j], 0x05040100u), p3 = __builtin_amdgcn_perm(wqv[3][j], wqv[2][j], 0x07060302u);
      a0[2 * j] = dot2(p2, x0[1], dot2(p0, x0[0], 0.f));
      a0[2 * j + 1] = dot2(p3, x0[1], dot2(p1, x0[0], 0.f));
      a1[2 * j] = dot2(p2, x1[1], dot2(p0, x1[0], 0.f));
      a1[2 * j + 1] = dot2(p3, x1[1], dot2(p1, x1[0], 0.f));
    }
    float z[8];
#pragma unroll
    for (int j = 0; j < 8; ++j) z[j] = swap32_add(a0[j], a1[j]);
    f32x4 u;
#pragma unroll
    for (int j = 0; j < 4; ++j) {
      float t = swap16_add(z[j], z[j + 4]);
      t += dppf<ROR4>(t);
      t += dppf<XOR8>(t);
      u[j] = t;
    }
    if ((lane & 12) == 0) *(f32x4*)(lds + L_QP + w * 64 + hh * 32 + (lane & 3) * 8 + ((lane >> 4) & 1) * 4) = u;
    ST(1);
    unsigned arrived = 0;
    asm volatile("s_waitcnt lgkmcnt(0)" : "+s"(tag)::"memory");
    if (lane == 0) arrived = __hip_atomic_fetch_add((unsigned*)lds + L_CNT, 1u, __ATOMIC_RELAXED, __HIP_MEMORY_SCOPE_WORKGROUP);
    asm volatile("" ::: "memory");
    if (__builtin_amdgcn_readfirstlane(arrived) == 7) {
      float q = 0.f;
#pragma unroll
      for (int i = 0; i < 8; ++i) q += lds[L_QP + i * 64 + lane];
      __builtin_amdgcn_raw_buffer_store_b32((pack_bf16(q, q) & 0xffffu) | (((lane & 1) ? tag : tag << 16) & 0xffff0000u), rw,
                                            WS_Q + (team * 2 + hh) * 1024 + ((lane >> 2) & 1) * 512 + (r * 4 + (l32 >> 3)) * 16 + (lane & 3) * 4, 0, AUX_SC1);
    }
    SB();
  };

  auto issue = LAMBDA(nc) {
    constexpr int n = decltype(nc)::value;
    if constexpr (n == N_WQ) {
      if (tid < 2) ((unsigned*)lds)[L_CNT + tid] = 0u;
      LDS_BARRIER();
      tag = *(const CAS unsigned*)((const CAS char*)prm.ws + WS_EPOCH + blk * 256) + 0x01010101u;
      if (bid < 8 && w == 7) __builtin_amdgcn_raw_buffer_store_b32(t_start, rw, WS_T + lane * 128 + bid * 4, 0, AUX_SC1);
      const unsigned xoff = lane < 56 ? (lane >> 2) * 8 : OOB;
      x0 = __builtin_amdgcn_raw_buffer_load_b64(rx, xoff, 0, 0);
      x1 = __builtin_amdgcn_raw_buffer_load_b64(rx, xoff, DM * 2, 0);
#pragma unroll
      for (int i = 0; i < 4; ++i) wqv[i] = bld<0>(rq, (lane >> 2) * 4 * (H * DH * 2) + (lane & 3) * 16, i * (H * DH * 2));
    }
    if constexpr (n == N_QCALC) q_share();
    if constexpr (n >= FREE_N) throttle<n>(t_start);
    if constexpr (n == N_QPOLL) qg = bld<AUX_SC1>(rw, WS_Q + (team * 2 + b) * 1024 + l16, 0);
    if constexpr (n == P_TN) tv = __builtin_amdgcn_raw_buffer_load_b32(rw, WS_T + (grp * 16 + h) * 128 + c8 * 4, 0, AUX_SC1);
    if constexpr (n == P_TN + 7) {
      int d = (int)(tv - t_start);
      d = (d > -400 && d < 400) ? d : 0;
      d += __builtin_amdgcn_mov_dpp(d, XOR1, 0xf, 0xf, true);
      d += __builtin_amdgcn_mov_dpp(d, XOR2, 0xf, 0xf, true);
      d += __builtin_amdgcn_mov_dpp(d, XOR7, 0xf, 0xf, true);
      t_start += (unsigned)((__builtin_amdgcn_readfirstlane(d) >> 3) + P_TOFF);
    }
    if constexpr (n == N_ML) ml = bld<AUX_SC1>(rw, WS_ML + (grp * 16 + h) * 512 + l32 * 16, 0);
    SB();
    if constexpr ((n & 1) == 0) {
      asm volatile("" : "+s"(kvb));
      kvo = (kvb + ((n & 2) << 14) + ((n & 60) << 15)) & (SK * DH * 2 - 1);
    }
    ring[n % WIN] = bld<AUX_NT>(n < NLD ? rk : rv, l32 * 16 + hh * 2048 + (n & 1) * 1024, kvo);
    SB();
    static_for<0, 32>(LAMBDA(jc) {
      constexpr int j = decltype(jc)::value;
      if constexpr (n == (j < 4 ? WM0 + j * WMS : WO0 + 3 * j)) {
        wb[j] = bld<0>(ro, 32 * rc * (DM * 2) + c8 * 16 + (j & 3) * (DM * 2), (unsigned)(((j >> 2) ^ r) * 4 * (DM * 2)));
        SB();
      }
    });
    constexpr int P0 = WO0 + DIST + 1;
    static_assert(P0 + 31 * 3 < 2 * NLD, "all wo rows are paired inside the stream");
    if constexpr (n >= P0 && (n - P0) % 3 == 0 && (n - P0) / 3 < 32 && (((n - P0) / 3) & 1)) {
      constexpr int j = (n - P0) / 3, t = j >> 1;
#pragma unroll
      for (int jj = 0; jj < 4; ++jj) {
        unsigned lo = __builtin_amdgcn_perm(wb[2 * t + 1][jj], wb[2 * t][jj], 0x05040100u);
        unsigned hi = __builtin_amdgcn_perm(wb[2 * t + 1][jj], wb[2 * t][jj], 0x07060302u);
        asm volatile("" : "+v"(lo), "+v"(hi));
        bt[2 * jj][t >> 3][t & 7] = lo;
        bt[2 * jj + 1][t >> 3][t & 7] = hi;
      }
      if constexpr (j == 31) {
#pragma unroll
        for (int jj = 0; jj < 8; ++jj) asm volatile("" : "+v"(bt[jj][0]), "+v"(bt[jj][1]));
      }
      SB();
    }
  };
  auto after = LAMBDA(gc) {
    constexpr int n = issue_after(decltype(gc)::value);
    SB();
    if constexpr (n >= 0) issue(std::integral_constant<int, n>{});
  };

  static_for<0, WIN>(issue);
  if (__builtin_expect(!__all(tagged16(qg, tag)), 0)) {
    for (unsigned spins = 0; spins < SPIN_CAP; ++spins) {
      asm volatile("" ::: "memory");
      qg = bld<AUX_SC1>(rw, WS_Q + (team * 2 + b) * 1024 + l16, 0);
      if (__all(tagged16(qg, tag))) break;
    }
  }
  v4u qv;
  {
    const unsigned q0 = __builtin_amdgcn_perm(qg[1], qg[0], 0x05040100u), q1 = __builtin_amdgcn_perm(qg[3], qg[2], 0x05040100u);
    auto e = __builtin_amdgcn_permlane32_swap(q0, q0, false, false);
    auto o = __builtin_amdgcn_permlane32_swap(q1, q1, false, false);
    qv = v4u{e[0], o[0], e[1], o[1]};
  }
  ST(2);

  float sc[2];
  {
    float xs[16];
    float red[2];
    static_for<0, NLD>(LAMBDA(tc) {
      constexpr int t = decltype(tc)::value;
      const v4u cur = ring[t % WIN];
      xs[t & 15] = dot2(cur[3], qv[3], dot2(cur[2], qv[2], dot2(cur[1], qv[1], dot2(cur[0], qv[0], 0.f))));
      after(tc);
      if constexpr ((t & 15) == 15) {
#define TR_STEP(HALF, CTRL)                                               \
  _Pragma("unroll") for (int u = 0; u < (HALF); ++u) {                    \
    const float lo_ = xs[u], hi_ = xs[u + (HALF)];                        \
    const float t1_ = lo_ + dppf<CTRL>(lo_), t2_ = hi_ + dppf<CTRL>(hi_); \
    xs[u] = (lane & (HALF)) ? t2_ : t1_;                                  \
  }
        TR_STEP(8, XOR8) TR_STEP(4, XOR7) TR_STEP(2, XOR2) TR_STEP(1, XOR1)
#undef TR_STEP
        asm volatile("" : "+v"(xs[0]));
        red[(t >> 4) & 1] = xs[0];
        if constexpr ((t & 31) == 31) sc[t >> 5] = swap16_add(red[0], red[1]);
        SB();
      }
    });
  }
  ST(3);
  constexpr float C2 = 0.0625f * 1.4426950408889634f;
  const float mw = swap32_max(half_max(fmaxf(sc[0], sc[1]))) * C2;
  {
    const float p0 = __builtin_amdgcn_exp2f(fmaf(sc[0], C2, -mw));
    const float p1 = __builtin_amdgcn_exp2f(fmaf(sc[1], C2, -mw));
    lds[L_P + w * 128 + hh * 64 + l32] = p0;
    lds[L_P + w * 128 + hh * 64 + 32 + l32] = p1;
    float lw = half_sum(p0 + p1);
    lw = swap32_add(lw, lw);
    unsigned arrived = 0;
    if (lane == 0) {
      *(f32x2*)(lds + L_ML + w * 2) = f32x2{mw, lw};
      asm volatile("" ::: "memory");
      arrived = __hip_atomic_fetch_add((unsigned*)lds + L_CNT + 1, 1u, __ATOMIC_RELAXED, __HIP_MEMORY_SCOPE_WORKGROUP);
    }
    asm volatile("" ::: "memory");
    if (__builtin_amdgcn_readfirstlane(arrived) == 7) {
      const v2u e = *(const v2u*)(lds + L_ML + c8 * 2);
      __builtin_amdgcn_raw_buffer_store_b128(v4u{__builtin_amdgcn_perm(tag, e[0], 0x05040100u), __builtin_amdgcn_perm(tag, e[0], 0x07060302u),
                                                 __builtin_amdgcn_perm(tag, e[1], 0x05040100u), __builtin_amdgcn_perm(tag, e[1], 0x07060302u)}, rw,
                                             (lane < 8 ? WS_ML : OOB) + (grp * 16 + h) * 512 + (s * 8 + c8) * 16, 0, AUX_SC1);
    }
  }
  unsigned yst = (w < 7 ? WS_Y : OOB) + ((grp * 14 + w * 2 + hh) * 64 + me) * 128 + l32 * 4;
  unsigned ytag = (tag >> ((lane & 3) * 8)) & 255u;
  unsigned accrd = L_ACC * 4 + rc * 1024 + w * 128 + c8 * 16;
  const int kk = c8 ^ r;
  unsigned owr = L_O * 4 + (((kk >> 2) * 4 + ((kk >> 1) & 1) * 2 + (w >> 2)) * 16 + hh * 8 + (w & 1) * 4 + ((w >> 1) & 1) * 2 + (kk & 1)) * 4;
  asm volatile("s_waitcnt lgkmcnt(0)" : "+v"(yst), "+v"(ytag), "+v"(accrd), "+v"(owr)::"memory");
  SB();

  f32x2 acc[4];
#pragma unroll
  for (int j = 0; j < 4; ++j) acc[j] = f32x2{0.f, 0.f};
  float fw;
  bool mlok;
  auto ml_eval = [&]() __attribute__((always_inline)) {
    mlok = __all(tagged16(ml, tag));
    const float m = __uint_as_float(__builtin_amdgcn_perm(ml[1], ml[0], 0x05040100u));
    const float M = half_max(m);
    const float L = half_sum(__builtin_amdgcn_exp2f(m - M) * __uint_as_float(__builtin_amdgcn_perm(ml[3], ml[2], 0x05040100u)));
    fw = __builtin_amdgcn_exp2f(mw - M) / L;
  };
  {
    const f32x4* pl = (const f32x4*)(lds + L_P + w * 128 + hh * 64);
    f32x4 pc = pl[0];
    static_for<0, NLD>(LAMBDA(tc) {
      constexpr int t = decltype(tc)::value;
      const v4u cur = ring[(t + NLD) % WIN];
      const float pv_ = pc[t & 3];
      if constexpr ((t & 3) == 3 && t + 1 < NLD) pc = pl[(t + 1) >> 2];
#pragma unroll
      for (int j = 0; j < 4; ++j) {
        const f32x2 vv = {__uint_as_float(cur[j] << 16), __uint_as_float(cur[j] & 0xffff0000u)};
        acc[j] = __builtin_elementwise_fma(vv, f32x2{pv_, pv_}, acc[j]);
      }
      asm volatile("" : "+v"(acc[0]), "+v"(acc[1]), "+v"(acc[2]), "+v"(acc[3]));
      after(std::integral_constant<int, t + NLD>{});
      if constexpr (t + NLD == G_ML) {
        ml_eval();
        asm volatile("" : "+v"(fw));
        SB();
      }
    });
  }
  if (__builtin_expect(!mlok, 0)) {
    for (unsigned spins = 0; !mlok && spins < SPIN_CAP; ++spins) {
      __builtin_amdgcn_s_sleep(8);
      asm volatile("" ::: "memory");
      ml = bld<AUX_SC1>(rw, WS_ML + (grp * 16 + h) * 512 + l32 * 16, 0);
      ml_eval();
    }
  }
  ST(4);

  {
    f32x4 rr;
#pragma unroll
    for (int j = 0; j < 4; ++j) rr[j] = swap32_add(acc[j >> 1][j & 1], acc[2 + (j >> 1)][j & 1]) * fw;
    *(f32x4*)(lds + L_ACC + w * 256 + l32 * 8 + hh * 4) = rr;
  }
  LDS_BARRIER();
  const f32x4 a = *(const f32x4*)((const char*)lds + accrd);
  SB();
  int sidx = (lane & 2) ? (int)0xEEEEEEEEu : 0x44444444;
  unsigned ord = L_O * 4 + (lane >> 4) * 64 + (lane & 3) * 16;
  f32x4 dd[8];
#pragma unroll
  for (int j = 0; j < 8; ++j) {
    dd[j] = f32x4{0.f, 0.f, 0.f, 0.f};
    asm volatile("" : "+v"(dd[j]));
  }
  asm volatile("" : "+v"(sidx), "+v"(ord));
  SB();
  {
    float e0 = swap32_add(a[0], a[2]), e1 = swap32_add(a[1], a[3]);
    e0 += dppf<XOR8>(e0);
    e1 += dppf<XOR8>(e1);
    e0 = swap16_add(e0, e0);
    e1 = swap16_add(e1, e1);
    if ((lane & 24) == 0) *(unsigned*)((char*)lds + owr) = pack_bf16(e0, e1);
  }
  LDS_BARRIER();

  {
    const v4u oa0 = *(const v4u*)((const char*)lds + ord), oa1 = *(const v4u*)((const char*)lds + ord + 256);
#pragma unroll
    for (int j = 0; j < 8; ++j)
      dd[j] = __builtin_amdgcn_smfmac_f32_16x16x64_bf16(__builtin_bit_cast(bf16x8, oa0), __builtin_bit_cast(bf16x16, bt[j][0]), dd[j], sidx, 0, 0);
#pragma unroll
    for (int j = 0; j < 8; ++j)
      dd[j] = __builtin_amdgcn_smfmac_f32_16x16x64_bf16(__builtin_bit_cast(bf16x8, oa1), __builtin_bit_cast(bf16x16, bt[j][1]), dd[j], sidx, 0, 0);
    SB();
    float y[8];
#pragma unroll
    for (int j = 0; j < 8; ++j) y[j] = sel(lane & 8, dd[j][1] + dd[j][3], dd[j][0] + dd[j][2]);
    float u[2];
#pragma unroll
    for (int j = 0; j < 2; ++j) u[j] = sel(hh, sel(lane & 16, y[6 + j], y[4 + j]), sel(lane & 16, y[2 + j], y[j]));
    const float t0 = u[0] + dppf<XOR8>(u[0]), t1 = u[1] + dppf<XOR8>(u[1]);
    const float yv = (lane & 8) ? t1 : t0;
    __builtin_amdgcn_raw_buffer_store_b32((__float_as_uint(yv) & ~255u) | ytag, rw, yst, 0, AUX_SC1);
    ST(5);
  }
  if (w < 7 || me >= 14) { ST_FLUSH(); return; }

  {
    const unsigned yo = WS_Y + (grp * 14 + me) * 8192 + l16;
    const v4u tg = {tag & 255u, (tag >> 8) & 255u, (tag >> 16) & 255u, tag >> 24};
    const int ls = 32 * (me & 1) + 4 * c8 + 2 * hh + ((lane >> 4) & 1);
    GAS unsigned short* optr = (GAS unsigned short*)prm.out + b * (2 * DM) + blk * DM + 64 * (me >> 1) + 8 * (ls & 7) + (ls >> 3);
    __builtin_amdgcn_s_sleep(16);
    f32x4 sum;
    for (unsigned spins = 0;; ++spins) {
      v4u g[8];
#pragma unroll
      for (int t = 0; t < 8; ++t) g[t] = bld<AUX_SC1>(rw, yo, t * 1024);
      unsigned bad = 0;
      sum = f32x4{0.f, 0.f, 0.f, 0.f};
#pragma unroll
      for (int t = 0; t < 8; ++t) {
        const v4u x = g[t] ^ tg;
        bad |= (x[0] | x[1]) | (x[2] | x[3]);
        sum += __builtin_bit_cast(f32x4, x);
      }
      if (__all((bad & 255u) == 0) || spins > SPIN_CAP) break;
      asm volatile("" ::: "memory");
    }
    ST(6);
    float rr = swap16_add(swap32_add(sum[0], sum[2]), swap32_add(sum[1], sum[3]));
    rr += dppf<XOR8>(rr);
    if ((lane & 8) == 0) __hip_atomic_store(optr, (unsigned short)(pack_bf16(rr, rr) & 0xffffu), RLX_AGENT);
    if (me == 0 && b == 0 && lane == 0) __hip_atomic_store((GAS unsigned*)((GAS char*)prm.ws + WS_EPOCH + blk * 256), tag, RLX_AGENT);
    {
      const unsigned tv2 = __builtin_amdgcn_raw_buffer_load_b32(rw, WS_T + (grp * 16 + h) * 128 + c8 * 4, 0, AUX_SC1);
      float d = (float)(int)(tv2 - t_start);
      float mx = fmaxf(d, dppf<XOR1>(d)); mx = fmaxf(mx, dppf<XOR2>(mx)); mx = fmaxf(mx, dppf<XOR7>(mx));
      float mn = fminf(d, dppf<XOR1>(d)); mn = fminf(mn, dppf<XOR2>(mn)); mn = fminf(mn, dppf<XOR7>(mn));
      float av = d + dppf<XOR1>(d); av += dppf<XOR2>(av); av += dppf<XOR7>(av); av *= 0.125f;
      int extra = (int)(10.f * (mx - (SK_MODE == 0 ? mn : av)));
      extra = extra < 0 ? 0 : extra > 20000 ? 20000 : extra;
      const int tgt = __builtin_amdgcn_readfirstlane((int)mx + 10000 + extra);
      while ((int)((unsigned)__builtin_amdgcn_s_memrealtime() - t_start) < tgt) __builtin_amdgcn_s_sleep(1);
    }
    ST(7);
    ST_FLUSH();
  }
}

extern "C" void kernel_launch(void* const* d_in, const int* in_sizes, int n_in, void* d_out, int out_size, void* d_ws,
                              size_t ws_size, hipStream_t stream) {
  Params p;
  p.x[0] = d_in[0];
  p.k[0] = d_in[1];
  p.v[0] = d_in[2];
  p.wq[0] = d_in[3];
  p.wo[0] = d_in[4];
  p.x[1] = d_in[5];
  p.k[1] = d_in[6];
  p.v[1] = d_in[7];
  p.wq[1] = d_in[8];
  p.wo[1] = d_in[9];
  p.out = d_out;
  p.ws = d_ws;
  mha2_decode_kernel<<<dim3(256), dim3(NTHR), 0, stream>>>(p);
}
```

```cpp
#define AMP
#include <hip/hip_runtime.h>
#include <hip/hip_bf16.h>
#include <stdint.h>
#include <type_traits>

#define GAS __attribute__((address_space(1)))
#define CAS __attribute__((address_space(4)))
typedef unsigned v2u __attribute__((ext_vector_type(2)));
typedef unsigned v4u __attribute__((ext_vector_type(4)));
typedef unsigned v8u __attribute__((ext_vector_type(8)));
typedef float f32x2 __attribute__((ext_vector_type(2)));
typedef float f32x4 __attribute__((ext_vector_type(4)));
typedef __bf16 bf16x2 __attribute__((ext_vector_type(2)));
typedef __bf16 bf16x8 __attribute__((ext_vector_type(8)));
typedef __bf16 bf16x16 __attribute__((ext_vector_type(16)));
#define RLX_AGENT __ATOMIC_RELAXED, __HIP_MEMORY_SCOPE_AGENT

constexpr int WIN = 16;
constexpr int DIST = 6;
constexpr int FREE_N = 4;
constexpr int N_WQ = 2;
constexpr int N_QCALC = 6;
constexpr int N_QPOLL = 13;
constexpr int WM0 = 6, WMS = 2;
constexpr int WO0 = 12;
constexpr int N_ML = 96;
constexpr int G_ML = 123;
constexpr int PACE_T0 = 30;
constexpr int PACE_DUR = 3560;
constexpr int PACE_GAIN = 128;

constexpr int H = 16, SK = 4096, DH = 256, DM = 448;
constexpr int NTHR = 512;
constexpr int NLD = 64;
constexpr unsigned WS_EPOCH = 0;
constexpr unsigned WS_Q = 4096;
constexpr unsigned WS_ML = WS_Q + 65536;
constexpr unsigned WS_Y = WS_ML + 32768;
constexpr unsigned WS_END = WS_Y + 4 * 14 * 8192;
constexpr unsigned SPIN_CAP = 1u << 20;
constexpr unsigned OOB = 0x7fffff00u;
constexpr int AUX_NT = 2, AUX_SC1 = 16;

struct Params {
  const void* k[2];
  const void* wq[2];
  const void* x[2];
  void* ws;
  void* out;
  const void* v[2];
  const void* wo[2];
};

__device__ __forceinline__ float dot2(unsigned a, unsigned b, float c) {
  return __builtin_amdgcn_fdot2_f32_bf16(__builtin_bit_cast(bf16x2, a), __builtin_bit_cast(bf16x2, b), c, false);
}
__device__ __forceinline__ unsigned pack_bf16(float lo, float hi) {
  bf16x2 r;
  r.x = (__bf16)lo;
  r.y = (__bf16)hi;
  return __builtin_bit_cast(unsigned, r);
}
template <int CTRL> __device__ __forceinline__ float dppf(float x) {
  return __builtin_bit_cast(float, __builtin_amdgcn_mov_dpp(__builtin_bit_cast(int, x), CTRL, 0xf, 0xf, true));
}
constexpr int XOR1 = 0xB1, XOR2 = 0x4E, XOR7 = 0x141, XOR8 = 0x128, ROR4 = 0x124;
__device__ __forceinline__ float swap16_add(float a, float b) {
  auto r = __builtin_amdgcn_permlane16_swap(__float_as_uint(a), __float_as_uint(b), false, false);
  return __uint_as_float(r[0]) + __uint_as_float(r[1]);
}
__device__ __forceinline__ float swap32_add(float a, float b) {
  auto r = __builtin_amdgcn_permlane32_swap(__float_as_uint(a), __float_as_uint(b), false, false);
  return __uint_as_float(r[0]) + __uint_as_float(r[1]);
}
__device__ __forceinline__ float swap32_max(float a) {
  auto r = __builtin_amdgcn_permlane32_swap(__float_as_uint(a), __float_as_uint(a), false, false);
  return fmaxf(__uint_as_float(r[0]), __uint_as_float(r[1]));
}
__device__ __forceinline__ float half_max(float v) {
  v = fmaxf(v, dppf<XOR1>(v));
  v = fmaxf(v, dppf<XOR2>(v));
  v = fmaxf(v, dppf<XOR7>(v));
  v = fmaxf(v, dppf<XOR8>(v));
  auto r = __builtin_amdgcn_permlane16_swap(__float_as_uint(v), __float_as_uint(v), false, false);
  return fmaxf(__uint_as_float(r[0]), __uint_as_float(r[1]));
}
__device__ __forceinline__ float half_sum(float v) {
  v += dppf<XOR1>(v);
  v += dppf<XOR2>(v);
  v += dppf<XOR7>(v);
  v += dppf<XOR8>(v);
  return swap16_add(v, v);
}
__device__ __forceinline__ float sel(bool c, float a, float b) {
  asm volatile("" : "+v"(a), "+v"(b));
  return c ? a : b;
}
__device__ __forceinline__ bool tagged16(v4u g, unsigned tag) {
  return __builtin_amdgcn_perm(g[1], g[0], 0x07060302u) == tag && __builtin_amdgcn_perm(g[3], g[2], 0x07060302u) == tag;
}
template <int I, int N, class F> __device__ __forceinline__ void static_for(F&& f) {
  if constexpr (I < N) {
    f(std::integral_constant<int, I>{});
    static_for<I + 1, N>(f);
  }
}
#define LAMBDA(v) [&](auto v) __attribute__((always_inline))
typedef __amdgpu_buffer_rsrc_t rsrc_t;
__device__ __forceinline__ rsrc_t mk_rsrc(const void* base, unsigned bytes) {
  return __builtin_amdgcn_make_buffer_rsrc((void*)base, 0, bytes, 0x00020000);
}
template <int AUX> __device__ __forceinline__ v4u bld(rsrc_t rs, unsigned voff, unsigned soff) {
  return __builtin_amdgcn_raw_buffer_load_b128(rs, voff, soff, AUX);
}
#define SB() __builtin_amdgcn_sched_barrier(0)
#define LDS_BARRIER()                                  \
  do {                                                 \
    asm volatile("s_waitcnt lgkmcnt(0)" ::: "memory"); \
    __builtin_amdgcn_s_barrier();                      \
    asm volatile("" ::: "memory");                     \
    SB();                                              \
  } while (0)

constexpr int pace_thr(int n, int depth) {
  const int dith16 = 2 * (((n & 1) << 2) | (n & 2) | ((n & 4) >> 2)) + 1;
  const int lag = depth == 2   ? -50
                  : depth <= 4 ? PACE_GAIN * (16 * (depth - 2) - dith16) / 16
                               : 2 * PACE_GAIN + (PACE_GAIN + 300 * n / 128) * (16 - dith16) / 16;
  return PACE_T0 + n * PACE_DUR / 128 + lag;
}
template <int N> __device__ __forceinline__ void throttle(unsigned t_start) {
  const int el = (int)((unsigned)__builtin_amdgcn_s_memrealtime() - t_start);
  asm volatile(
      "s_waitcnt vmcnt(5)\n\ts_cmpk_ge_i32 %0, %1\n\ts_cbranch_scc1 1f\n\ts_waitcnt vmcnt(3)\n\t"
      "s_cmpk_ge_i32 %0, %2\n\ts_cbranch_scc1 1f\n\ts_waitcnt vmcnt(2)\n\t"
      "s_cmpk_ge_i32 %0, %3\n\ts_cbranch_scc1 1f\n\ts_waitcnt vmcnt(1)\n\t"
      "s_cmpk_ge_i32 %0, %4\n\ts_cbranch_scc1 1f\n\ts_waitcnt vmcnt(0)\n1:" ::"s"(el),
      "i"(pace_thr(N, 5)), "i"(pace_thr(N, 4)), "i"(pace_thr(N, 3)), "i"(pace_thr(N, 2))
      : "scc");
}
constexpr int issue_after(int g) {
  const int n = g < 2 * (WIN - DIST) ? ((g & 1) ? WIN + g / 2 : -1) : g + DIST;
  return n < 2 * NLD ? n : -1;
}

#define ST(k)
#define ST_FLUSH()

constexpr int L_QP = 0;
constexpr int L_P = L_QP + 8 * 64;
constexpr int L_ACC = L_P + 8 * 128;
constexpr int L_O = L_ACC + 8 * 256;
constexpr int L_ML = L_O + 128;
constexpr int L_CNT = L_ML + 16;
constexpr int L_END = L_CNT + 4;

__global__ void __launch_bounds__(NTHR, 2) mha2_decode_kernel(Params prm) {
  __shared__ __attribute__((aligned(16))) float lds[L_END];
  ST(0);
  const unsigned t_start = (unsigned)__builtin_amdgcn_s_memrealtime();
  const int tid = threadIdx.x, lane = tid & 63;
  const int w = __builtin_amdgcn_readfirstlane(tid >> 6);
  const int bid = blockIdx.x;
  const int team = (bid & 7) * 4 + (bid >> 6);
  const int blk = team >> 4, h = team & 15;
  const int r = (bid >> 3) & 7, b = r >> 2, s = r & 3;
  const int hh = lane >> 5, l32 = lane & 31, rc = lane >> 3, c8 = lane & 7;
  const int grp = blk * 2 + b, me = s * 16 + h;
  const char* const pk = (const char*)(blk ? prm.k[1] : prm.k[0]);
  const char* const pv = (const char*)(blk ? prm.v[1] : prm.v[0]);
  const char* const pwq = (const char*)(blk ? prm.wq[1] : prm.wq[0]);
  const char* const pwo = (const char*)(blk ? prm.wo[1] : prm.wo[0]);
  const char* const px = (const char*)(blk ? prm.x[1] : prm.x[0]);

  const size_t kvoff = (size_t)(b * H + h) * SK * DH * 2;
  const rsrc_t rk = mk_rsrc(pk + kvoff, SK * DH * 2);
  const rsrc_t rv = mk_rsrc(pv + kvoff, SK * DH * 2);
  unsigned kvb = (unsigned)(grp * 16 + h) * 32768u + ((w & 1) << 9) + ((w & 6) << 11) + ((s & 1) << 14) + ((s & 2) << 15);
  unsigned kvo = 0;
  const unsigned l16 = lane * 16;
  const rsrc_t rw = mk_rsrc(prm.ws, WS_END);
  const rsrc_t rq = mk_rsrc(pwq + (size_t)(w * 56) * (H * DH * 2) + h * (DH * 2) + r * 64, 55 * (H * DH * 2) + 64);
  const rsrc_t rx = mk_rsrc(px + w * 112, DM * 2 + 112);
  const rsrc_t ro = mk_rsrc(pwo + (size_t)h * DH * (DM * 2) + w * 128, w < 7 ? 255 * (DM * 2) + 128 : 0);

  unsigned tag;
  v4u ring[WIN];
  v4u wqv[4], qg, ml;
  v2u x0, x1;
  v4u wb[32];
  v8u bt[8][2];

  auto q_share = [&]() __attribute__((always_inline)) {
    float a0[8], a1[8];
#pragma unroll
    for (int j = 0; j < 4; ++j) {
      const unsigned p0 = __builtin_amdgcn_perm(wqv[1][j], wqv[0][j], 0x05040100u), p1 = __builtin_amdgcn_perm(wqv[1][j], wqv[0][j], 0x07060302u);
      const unsigned p2 = __builtin_amdgcn_perm(wqv[3][j], wqv[2][j], 0x05040100u), p3 = __builtin_amdgcn_perm(wqv[3][j], wqv[2][j], 0x07060302u);
      a0[2 * j] = dot2(p2, x0[1], dot2(p0, x0[0], 0.f));
      a0[2 * j + 1] = dot2(p3, x0[1], dot2(p1, x0[0], 0.f));
      a1[2 * j] = dot2(p2, x1[1], dot2(p0, x1[0], 0.f));
      a1[2 * j + 1] = dot2(p3, x1[1], dot2(p1, x1[0], 0.f));
    }
    float z[8];
#pragma unroll
    for (int j = 0; j < 8; ++j) z[j] = swap32_add(a0[j], a1[j]);
    f32x4 u;
#pragma unroll
    for (int j = 0; j < 4; ++j) {
      float t = swap16_add(z[j], z[j + 4]);
      t += dppf<ROR4>(t);
      t += dppf<XOR8>(t);
      u[j] = t;
    }
    if ((lane & 12) == 0) *(f32x4*)(lds + L_QP + w * 64 + hh * 32 + (lane & 3) * 8 + ((lane >> 4) & 1) * 4) = u;
    ST(1);
    unsigned arrived = 0;
    asm volatile("s_waitcnt lgkmcnt(0)" : "+s"(tag)::"memory");
    if (lane == 0) arrived = __hip_atomic_fetch_add((unsigned*)lds + L_CNT, 1u, __ATOMIC_RELAXED, __HIP_MEMORY_SCOPE_WORKGROUP);
    asm volatile("" ::: "memory");
    if (__builtin_amdgcn_readfirstlane(arrived) == 7) {
      float q = 0.f;
#pragma unroll
      for (int i = 0; i < 8; ++i) q += lds[L_QP + i * 64 + lane];
      __builtin_amdgcn_raw_buffer_store_b32((pack_bf16(q, q) & 0xffffu) | (((lane & 1) ? tag : tag << 16) & 0xffff0000u), rw,
                                            WS_Q + (team * 2 + hh) * 1024 + ((lane >> 2) & 1) * 512 + (r * 4 + (l32 >> 3)) * 16 + (lane & 3) * 4, 0, AUX_SC1);
    }
    SB();
  };

  auto issue = LAMBDA(nc) {
    constexpr int n = decltype(nc)::value;
    if constexpr (n == N_WQ) {
      if (tid < 2) ((unsigned*)lds)[L_CNT + tid] = 0u;
      LDS_BARRIER();
      tag = *(const CAS unsigned*)((const CAS char*)prm.ws + WS_EPOCH + blk * 256) + 0x01010101u;
      const unsigned xoff = lane < 56 ? (lane >> 2) * 8 : OOB;
      x0 = __builtin_amdgcn_raw_buffer_load_b64(rx, xoff, 0, 0);
      x1 = __builtin_amdgcn_raw_buffer_load_b64(rx, xoff, DM * 2, 0);
#pragma unroll
      for (int i = 0; i < 4; ++i) wqv[i] = bld<0>(rq, (lane >> 2) * 4 * (H * DH * 2) + (lane & 3) * 16, i * (H * DH * 2));
    }
    if constexpr (n == N_QCALC) q_share();
    if constexpr (n >= FREE_N) throttle<n>(t_start);
    if constexpr (n == N_QPOLL) qg = bld<AUX_SC1>(rw, WS_Q + (team * 2 + b) * 1024 + l16, 0);
    if constexpr (n == N_ML) ml = bld<AUX_SC1>(rw, WS_ML + (grp * 16 + h) * 512 + l32 * 16, 0);
    SB();
    if constexpr ((n & 1) == 0) {
      asm volatile("" : "+s"(kvb));
      kvo = (kvb + ((n & 2) << 14) + ((n & 60) << 15)) & (SK * DH * 2 - 1);
    }
    ring[n % WIN] = bld<AUX_NT>(n < NLD ? rk : rv, l32 * 16 + hh * 2048 + (n & 1) * 1024, kvo);
    SB();
    static_for<0, 32>(LAMBDA(jc) {
      constexpr int j = decltype(jc)::value;
      if constexpr (n == (j < 4 ? WM0 + j * WMS : WO0 + 3 * j)) {
        wb[j] = bld<0>(ro, 32 * rc * (DM * 2) + c8 * 16 + (j & 3) * (DM * 2), (unsigned)(((j >> 2) ^ r) * 4 * (DM * 2)));
        SB();
      }
    });
    constexpr int P0 = WO0 + DIST + 1;
    static_assert(P0 + 31 * 3 < 2 * NLD, "all wo rows are paired inside the stream");
    if constexpr (n >= P0 && (n - P0) % 3 == 0 && (n - P0) / 3 < 32 && (((n - P0) / 3) & 1)) {
      constexpr int j = (n - P0) / 3, t = j >> 1;
#pragma unroll
      for (int jj = 0; jj < 4; ++jj) {
        unsigned lo = __builtin_amdgcn_perm(wb[2 * t + 1][jj], wb[2 * t][jj], 0x05040100u);
        unsigned hi = __builtin_amdgcn_perm(wb[2 * t + 1][jj], wb[2 * t][jj], 0x07060302u);
        asm volatile("" : "+v"(lo), "+v"(hi));
        bt[2 * jj][t >> 3][t & 7] = lo;
        bt[2 * jj + 1][t >> 3][t & 7] = hi;
      }
      if constexpr (j == 31) {
#pragma unroll
        for (int jj = 0; jj < 8; ++jj) asm volatile("" : "+v"(bt[jj][0]), "+v"(bt[jj][1]));
      }
      SB();
    }
  };
  auto after = LAMBDA(gc) {
    constexpr int n = issue_after(decltype(gc)::value);
    SB();
    if constexpr (n >= 0) issue(std::integral_constant<int, n>{});
  };

  static_for<0, WIN>(issue);
  if (__builtin_expect(!__all(tagged16(qg, tag)), 0)) {
    for (unsigned spins = 0; spins < SPIN_CAP; ++spins) {
      asm volatile("" ::: "memory");
      qg = bld<AUX_SC1>(rw, WS_Q + (team * 2 + b) * 1024 + l16, 0);
      if (__all(tagged16(qg, tag))) break;
    }
  }
  v4u qv;
  {
    const unsigned q0 = __builtin_amdgcn_perm(qg[1], qg[0], 0x05040100u), q1 = __builtin_amdgcn_perm(qg[3], qg[2], 0x05040100u);
    auto e = __builtin_amdgcn_permlane32_swap(q0, q0, false, false);
    auto o = __builtin_amdgcn_permlane32_swap(q1, q1, false, false);
    qv = v4u{e[0], o[0], e[1], o[1]};
  }
  ST(2);

  float sc[2];
  {
    float xs[16];
    float red[2];
    static_for<0, NLD>(LAMBDA(tc) {
      constexpr int t = decltype(tc)::value;
      const v4u cur = ring[t % WIN];
      xs[t & 15] = dot2(cur[3], qv[3], dot2(cur[2], qv[2], dot2(cur[1], qv[1], dot2(cur[0], qv[0], 0.f))));
      after(tc);
      if constexpr ((t & 15) == 15) {
#define TR_STEP(HALF, CTRL)                                               \
  _Pragma("unroll") for (int u = 0; u < (HALF); ++u) {                    \
    const float lo_ = xs[u], hi_ = xs[u + (HALF)];                        \
    const float t1_ = lo_ + dppf<CTRL>(lo_), t2_ = hi_ + dppf<CTRL>(hi_); \
    xs[u] = (lane & (HALF)) ? t2_ : t1_;                                  \
  }
        TR_STEP(8, XOR8) TR_STEP(4, XOR7) TR_STEP(2, XOR2) TR_STEP(1, XOR1)
#undef TR_STEP
        asm volatile("" : "+v"(xs[0]));
        red[(t >> 4) & 1] = xs[0];
        if constexpr ((t & 31) == 31) sc[t >> 5] = swap16_add(red[0], red[1]);
        SB();
      }
    });
  }
  ST(3);
  constexpr float C2 = 0.0625f * 1.4426950408889634f;
  const float mw = swap32_max(half_max(fmaxf(sc[0], sc[1]))) * C2;
  {
    const float p0 = __builtin_amdgcn_exp2f(fmaf(sc[0], C2, -mw));
    const float p1 = __builtin_amdgcn_exp2f(fmaf(sc[1], C2, -mw));
    lds[L_P + w * 128 + hh * 64 + l32] = p0;
    lds[L_P + w * 128 + hh * 64 + 32 + l32] = p1;
    float lw = half_sum(p0 + p1);
    lw = swap32_add(lw, lw);
    unsigned arrived = 0;
    if (lane == 0) {
      *(f32x2*)(lds + L_ML + w * 2) = f32x2{mw, lw};
      asm volatile("" ::: "memory");
      arrived = __hip_atomic_fetch_add((unsigned*)lds + L_CNT + 1, 1u, __ATOMIC_RELAXED, __HIP_MEMORY_SCOPE_WORKGROUP);
    }
    asm volatile("" ::: "memory");
    if (__builtin_amdgcn_readfirstlane(arrived) == 7) {
      const v2u e = *(const v2u*)(lds + L_ML + c8 * 2);
      __builtin_amdgcn_raw_buffer_store_b128(v4u{__builtin_amdgcn_perm(tag, e[0], 0x05040100u), __builtin_amdgcn_perm(tag, e[0], 0x07060302u),
                                                 __builtin_amdgcn_perm(tag, e[1], 0x05040100u), __builtin_amdgcn_perm(tag, e[1], 0x07060302u)}, rw,
                                             (lane < 8 ? WS_ML : OOB) + (grp * 16 + h) * 512 + (s * 8 + c8) * 16, 0, AUX_SC1);
    }
  }
  unsigned yst = (w < 7 ? WS_Y : OOB) + ((grp * 14 + w * 2 + hh) * 64 + me) * 128 + l32 * 4;
  unsigned ytag = (tag >> ((lane & 3) * 8)) & 255u;
  unsigned accrd = L_ACC * 4 + rc * 1024 + w * 128 + c8 * 16;
  const int kk = c8 ^ r;
  unsigned owr = L_O * 4 + (((kk >> 2) * 4 + ((kk >> 1) & 1) * 2 + (w >> 2)) * 16 + hh * 8 + (w & 1) * 4 + ((w >> 1) & 1) * 2 + (kk & 1)) * 4;
  asm volatile("s_waitcnt lgkmcnt(0)" : "+v"(yst), "+v"(ytag), "+v"(accrd), "+v"(owr)::"memory");
  SB();

  f32x2 acc[4];
#pragma unroll
  for (int j = 0; j < 4; ++j) acc[j] = f32x2{0.f, 0.f};
  float fw;
  bool mlok;
  auto ml_eval = [&]() __attribute__((always_inline)) {
    mlok = __all(tagged16(ml, tag));
    const float m = __uint_as_float(__builtin_amdgcn_perm(ml[1], ml[0], 0x05040100u));
    const float M = half_max(m);
    const float L = half_sum(__builtin_amdgcn_exp2f(m - M) * __uint_as_float(__builtin_amdgcn_perm(ml[3], ml[2], 0x05040100u)));
    fw = __builtin_amdgcn_exp2f(mw - M) / L;
  };
  {
    const f32x4* pl = (const f32x4*)(lds + L_P + w * 128 + hh * 64);
    f32x4 pc = pl[0];
    static_for<0, NLD>(LAMBDA(tc) {
      constexpr int t = decltype(tc)::value;
      const v4u cur = ring[(t + NLD) % WIN];
      const float pv_ = pc[t & 3];
      if constexpr ((t & 3) == 3 && t + 1 < NLD) pc = pl[(t + 1) >> 2];
#pragma unroll
      for (int j = 0; j < 4; ++j) {
        const f32x2 vv = {__uint_as_float(cur[j] << 16), __uint_as_float(cur[j] & 0xffff0000u)};
        acc[j] = __builtin_elementwise_fma(vv, f32x2{pv_, pv_}, acc[j]);
      }
      asm volatile("" : "+v"(acc[0]), "+v"(acc[1]), "+v"(acc[2]), "+v"(acc[3]));
      after(std::integral_constant<int, t + NLD>{});
      if constexpr (t + NLD == G_ML) {
        ml_eval();
        asm volatile("" : "+v"(fw));
        SB();
      }
    });
  }
  if (__builtin_expect(!mlok, 0)) {
    for (unsigned spins = 0; !mlok && spins < SPIN_CAP; ++spins) {
      __builtin_amdgcn_s_sleep(8);
      asm volatile("" ::: "memory");
      ml = bld<AUX_SC1>(rw, WS_ML + (grp * 16 + h) * 512 + l32 * 16, 0);
      ml_eval();
    }
  }
  ST(4);

  {
    f32x4 rr;
#pragma unroll
    for (int j = 0; j < 4; ++j) rr[j] = swap32_add(acc[j >> 1][j & 1], acc[2 + (j >> 1)][j & 1]) * fw;
    *(f32x4*)(lds + L_ACC + w * 256 + l32 * 8 + hh * 4) = rr;
  }
  LDS_BARRIER();
  const f32x4 a = *(const f32x4*)((const char*)lds + accrd);
  SB();
  int sidx = (lane & 2) ? (int)0xEEEEEEEEu : 0x44444444;
  unsigned ord = L_O * 4 + (lane >> 4) * 64 + (lane & 3) * 16;
  f32x4 dd[8];
#pragma unroll
  for (int j = 0; j < 8; ++j) {
    dd[j] = f32x4{0.f, 0.f, 0.f, 0.f};
    asm volatile("" : "+v"(dd[j]));
  }
  asm volatile("" : "+v"(sidx), "+v"(ord));
  SB();
  {
    float e0 = swap32_add(a[0], a[2]), e1 = swap32_add(a[1], a[3]);
    e0 += dppf<XOR8>(e0);
    e1 += dppf<XOR8>(e1);
    e0 = swap16_add(e0, e0);
    e1 = swap16_add(e1, e1);
    if ((lane & 24) == 0) *(unsigned*)((char*)lds + owr) = pack_bf16(e0, e1);
  }
  LDS_BARRIER();

  {
    const v4u oa0 = *(const v4u*)((const char*)lds + ord), oa1 = *(const v4u*)((const char*)lds + ord + 256);
#pragma unroll
    for (int j = 0; j < 8; ++j)
      dd[j] = __builtin_amdgcn_smfmac_f32_16x16x64_bf16(__builtin_bit_cast(bf16x8, oa0), __builtin_bit_cast(bf16x16, bt[j][0]), dd[j], sidx, 0, 0);
#pragma unroll
    for (int j = 0; j < 8; ++j)
      dd[j] = __builtin_amdgcn_smfmac_f32_16x16x64_bf16(__builtin_bit_cast(bf16x8, oa1), __builtin_bit_cast(bf16x16, bt[j][1]), dd[j], sidx, 0, 0);
    SB();
    float y[8];
#pragma unroll
    for (int j = 0; j < 8; ++j) y[j] = sel(lane & 8, dd[j][1] + dd[j][3], dd[j][0] + dd[j][2]);
    float u[2];
#pragma unroll
    for (int j = 0; j < 2; ++j) u[j] = sel(hh, sel(lane & 16, y[6 + j], y[4 + j]), sel(lane & 16, y[2 + j], y[j]));
    const float t0 = u[0] + dppf<XOR8>(u[0]), t1 = u[1] + dppf<XOR8>(u[1]);
    const float yv = (lane & 8) ? t1 : t0;
    __builtin_amdgcn_raw_buffer_store_b32((__float_as_uint(yv) & ~255u) | ytag, rw, yst, 0, AUX_SC1);
    ST(5);
  }
  if (w < 7 || me >= 14) { ST_FLUSH(); return; }

  {
    const unsigned yo = WS_Y + (grp * 14 + me) * 8192 + l16;
    const v4u tg = {tag & 255u, (tag >> 8) & 255u, (tag >> 16) & 255u, tag >> 24};
    const int ls = 32 * (me & 1) + 4 * c8 + 2 * hh + ((lane >> 4) & 1);
    GAS unsigned short* optr = (GAS unsigned short*)prm.out + b * (2 * DM) + blk * DM + 64 * (me >> 1) + 8 * (ls & 7) + (ls >> 3);
    __builtin_amdgcn_s_sleep(16);
    f32x4 sum;
    for (unsigned spins = 0;; ++spins) {
      v4u g[8];
#pragma unroll
      for (int t = 0; t < 8; ++t) g[t] = bld<AUX_SC1>(rw, yo, t * 1024);
      unsigned bad = 0;
      sum = f32x4{0.f, 0.f, 0.f, 0.f};
#pragma unroll
      for (int t = 0; t < 8; ++t) {
        const v4u x = g[t] ^ tg;
        bad |= (x[0] | x[1]) | (x[2] | x[3]);
        sum += __builtin_bit_cast(f32x4, x);
      }
      if (__all((bad & 255u) == 0) || spins > SPIN_CAP) break;
      asm volatile("" ::: "memory");
    }
    ST(6);
    float rr = swap16_add(swap32_add(sum[0], sum[2]), swap32_add(sum[1], sum[3]));
    rr += dppf<XOR8>(rr);
    if ((lane & 8) == 0) __hip_atomic_store(optr, (unsigned short)(pack_bf16(rr, rr) & 0xffffu), RLX_AGENT);
    if (me == 0 && b == 0 && lane == 0) __hip_atomic_store((GAS unsigned*)((GAS char*)prm.ws + WS_EPOCH + blk * 256), tag, RLX_AGENT);
    {
      const int el = (int)((unsigned)__builtin_amdgcn_s_memrealtime() - t_start);
      const int tgt = 10000 + 5 * (el - 3800);
      while ((int)((unsigned)__builtin_amdgcn_s_memrealtime() - t_start) < tgt) __builtin_amdgcn_s_sleep(1);
    }
    ST(7);
    ST_FLUSH();
  }
}

extern "C" void kernel_launch(void* const* d_in, const int* in_sizes, int n_in, void* d_out, int out_size, void* d_ws,
                              size_t ws_size, hipStream_t stream) {
  Params p;
  p.x[0] = d_in[0];
  p.k[0] = d_in[1];
  p.v[0] = d_in[2];
  p.wq[0] = d_in[3];
  p.wo[0] = d_in[4];
  p.x[1] = d_in[5];
  p.k[1] = d_in[6];
  p.v[1] = d_in[7];
  p.wq[1] = d_in[8];
  p.wo[1] = d_in[9];
  p.out = d_out;
  p.ws = d_ws;
  mha2_decode_kernel<<<dim3(256), dim3(NTHR), 0, stream>>>(p);
}
```

```cpp
#define AMP
#define LTOUCH 30
#include <hip/hip_runtime.h>
#include <hip/hip_bf16.h>
#include <stdint.h>
#include <type_traits>

#define GAS __attribute__((address_space(1)))
#define CAS __attribute__((address_space(4)))
typedef unsigned v2u __attribute__((ext_vector_type(2)));
typedef unsigned v4u __attribute__((ext_vector_type(4)));
typedef unsigned v8u __attribute__((ext_vector_type(8)));
typedef float f32x2 __attribute__((ext_vector_type(2)));
typedef float f32x4 __attribute__((ext_vector_type(4)));
typedef __bf16 bf16x2 __attribute__((ext_vector_type(2)));
typedef __bf16 bf16x8 __attribute__((ext_vector_type(8)));
typedef __bf16 bf16x16 __attribute__((ext_vector_type(16)));
#define RLX_AGENT __ATOMIC_RELAXED, __HIP_MEMORY_SCOPE_AGENT

constexpr int WIN = 16;
constexpr int DIST = 6;
constexpr int FREE_N = 4;
constexpr int N_WQ = 2;
constexpr int N_QCALC = 6;
constexpr int N_QPOLL = 13;
constexpr int WM0 = 6, WMS = 2;
constexpr int WO0 = 12;
constexpr int N_ML = 96;
constexpr int G_ML = 123;
constexpr int PACE_T0 = 30;
constexpr int PACE_DUR = 3560;
constexpr int PACE_GAIN = 128;

constexpr int H = 16, SK = 4096, DH = 256, DM = 448;
constexpr int NTHR = 512;
constexpr int NLD = 64;
constexpr unsigned WS_EPOCH = 0;
constexpr unsigned WS_Q = 4096;
constexpr unsigned WS_ML = WS_Q + 65536;
constexpr unsigned WS_Y = WS_ML + 32768;
constexpr unsigned WS_END = WS_Y + 4 * 14 * 8192;
constexpr unsigned SPIN_CAP = 1u << 20;
constexpr unsigned OOB = 0x7fffff00u;
constexpr int AUX_NT = 2, AUX_SC1 = 16;

struct Params {
  const void* k[2];
  const void* wq[2];
  const void* x[2];
  void* ws;
  void* out;
  const void* v[2];
  const void* wo[2];
};

__device__ __forceinline__ float dot2(unsigned a, unsigned b, float c) {
  return __builtin_amdgcn_fdot2_f32_bf16(__builtin_bit_cast(bf16x2, a), __builtin_bit_cast(bf16x2, b), c, false);
}
__device__ __forceinline__ unsigned pack_bf16(float lo, float hi) {
  bf16x2 r;
  r.x = (__bf16)lo;
  r.y = (__bf16)hi;
  return __builtin_bit_cast(unsigned, r);
}
template <int CTRL> __device__ __forceinline__ float dppf(float x) {
  return __builtin_bit_cast(float, __builtin_amdgcn_mov_dpp(__builtin_bit_cast(int, x), CTRL, 0xf, 0xf, true));
}
constexpr int XOR1 = 0xB1, XOR2 = 0x4E, XOR7 = 0x141, XOR8 = 0x128, ROR4 = 0x124;
__device__ __forceinline__ float swap16_add(float a, float b) {
  auto r = __builtin_amdgcn_permlane16_swap(__float_as_uint(a), __float_as_uint(b), false, false);
  return __uint_as_float(r[0]) + __uint_as_float(r[1]);
}
__device__ __forceinline__ float swap32_add(float a, float b) {
  auto r = __builtin_amdgcn_permlane32_swap(__float_as_uint(a), __float_as_uint(b), false, false);
  return __uint_as_float(r[0]) + __uint_as_float(r[1]);
}
__device__ __forceinline__ float swap32_max(float a) {
  auto r = __builtin_amdgcn_permlane32_swap(__float_as_uint(a), __float_as_uint(a), false, false);
  return fmaxf(__uint_as_float(r[0]), __uint_as_float(r[1]));
}
__device__ __forceinline__ float half_max(float v) {
  v = fmaxf(v, dppf<XOR1>(v));
  v = fmaxf(v, dppf<XOR2>(v));
  v = fmaxf(v, dppf<XOR7>(v));
  v = fmaxf(v, dppf<XOR8>(v));
  auto r = __builtin_amdgcn_permlane16_swap(__float_as_uint(v), __float_as_uint(v), false, false);
  return fmaxf(__uint_as_float(r[0]), __uint_as_float(r[1]));
}
__device__ __forceinline__ float half_sum(float v) {
  v += dppf<XOR1>(v);
  v += dppf<XOR2>(v);
  v += dppf<XOR7>(v);
  v += dppf<XOR8>(v);
  return swap16_add(v, v);
}
__device__ __forceinline__ float sel(bool c, float a, float b) {
  asm volatile("" : "+v"(a), "+v"(b));
  return c ? a : b;
}
__device__ __forceinline__ bool tagged16(v4u g, unsigned tag) {
  return __builtin_amdgcn_perm(g[1], g[0], 0x07060302u) == tag && __builtin_amdgcn_perm(g[3], g[2], 0x07060302u) == tag;
}
template <int I, int N, class F> __device__ __forceinline__ void static_for(F&& f) {
  if constexpr (I < N) {
    f(std::integral_constant<int, I>{});
    static_for<I + 1, N>(f);
  }
}
#define LAMBDA(v) [&](auto v) __attribute__((always_inline))
typedef __amdgpu_buffer_rsrc_t rsrc_t;
__device__ __forceinline__ rsrc_t mk_rsrc(const void* base, unsigned bytes) {
  return __builtin_amdgcn_make_buffer_rsrc((void*)base, 0, bytes, 0x00020000);
}
template <int AUX> __device__ __forceinline__ v4u bld(rsrc_t rs, unsigned voff, unsigned soff) {
  return __builtin_amdgcn_raw_buffer_load_b128(rs, voff, soff, AUX);
}
#define SB() __builtin_amdgcn_sched_barrier(0)
#define LDS_BARRIER()                                  \
  do {                                                 \
    asm volatile("s_waitcnt lgkmcnt(0)" ::: "memory"); \
    __builtin_amdgcn_s_barrier();                      \
    asm volatile("" ::: "memory");                     \
    SB();                                              \
  } while (0)

constexpr int pace_thr(int n, int depth) {
  const int dith16 = 2 * (((n & 1) << 2) | (n & 2) | ((n & 4) >> 2)) + 1;
  const int lag = depth == 2   ? -50
                  : depth <= 4 ? PACE_GAIN * (16 * (depth - 2) - dith16) / 16
                               : 2 * PACE_GAIN + (PACE_GAIN + 300 * n / 128) * (16 - dith16) / 16;
  return PACE_T0 + n * PACE_DUR / 128 + lag;
}
template <int N> __device__ __forceinline__ void throttle(unsigned t_start) {
  const int el = (int)((unsigned)__builtin_amdgcn_s_memrealtime() - t_start);
  asm volatile(
      "s_waitcnt vmcnt(5)\n\ts_cmpk_ge_i32 %0, %1\n\ts_cbranch_scc1 1f\n\ts_waitcnt vmcnt(3)\n\t"
      "s_cmpk_ge_i32 %0, %2\n\ts_cbranch_scc1 1f\n\ts_waitcnt vmcnt(2)\n\t"
      "s_cmpk_ge_i32 %0, %3\n\ts_cbranch_scc1 1f\n\ts_waitcnt vmcnt(1)\n\t"
      "s_cmpk_ge_i32 %0, %4\n\ts_cbranch_scc1 1f\n\ts_waitcnt vmcnt(0)\n1:" ::"s"(el),
      "i"(pace_thr(N, 5)), "i"(pace_thr(N, 4)), "i"(pace_thr(N, 3)), "i"(pace_thr(N, 2))
      : "scc");
}
constexpr int issue_after(int g) {
  const int n = g < 2 * (WIN - DIST) ? ((g & 1) ? WIN + g / 2 : -1) : g + DIST;
  return n < 2 * NLD ? n : -1;
}

#define ST(k)
#define ST_FLUSH()

constexpr int L_QP = 0;
constexpr int L_P = L_QP + 8 * 64;
constexpr int L_ACC = L_P + 8 * 128;
constexpr int L_O = L_ACC + 8 * 256;
constexpr int L_ML = L_O + 128;
constexpr int L_CNT = L_ML + 16;
constexpr int L_END = L_CNT + 4;

__global__ void __launch_bounds__(NTHR, 2) mha2_decode_kernel(Params prm) {
  __shared__ __attribute__((aligned(16))) float lds[L_END];
  ST(0);
  const unsigned t_start = (unsigned)__builtin_amdgcn_s_memrealtime();
  const int tid = threadIdx.x, lane = tid & 63;
  const int w = __builtin_amdgcn_readfirstlane(tid >> 6);
  const int bid = blockIdx.x;
  const int team = (bid & 7) * 4 + (bid >> 6);
  const int blk = team >> 4, h = team & 15;
  const int r = (bid >> 3) & 7, b = r >> 2, s = r & 3;
  const int hh = lane >> 5, l32 = lane & 31, rc = lane >> 3, c8 = lane & 7;
  const int grp = blk * 2 + b, me = s * 16 + h;
  const char* const pk = (const char*)(blk ? prm.k[1] : prm.k[0]);
  const char* const pv = (const char*)(blk ? prm.v[1] : prm.v[0]);
  const char* const pwq = (const char*)(blk ? prm.wq[1] : prm.wq[0]);
  const char* const pwo = (const char*)(blk ? prm.wo[1] : prm.wo[0]);
  const char* const px = (const char*)(blk ? prm.x[1] : prm.x[0]);

  const size_t kvoff = (size_t)(b * H + h) * SK * DH * 2;
  const rsrc_t rk = mk_rsrc(pk + kvoff, SK * DH * 2);
  const rsrc_t rv = mk_rsrc(pv + kvoff, SK * DH * 2);
  unsigned kvb = (unsigned)(grp * 16 + h) * 32768u + ((w & 1) << 9) + ((w & 6) << 11) + ((s & 1) << 14) + ((s & 2) << 15);
  unsigned kvo = 0;
  const unsigned l16 = lane * 16;
  const rsrc_t rw = mk_rsrc(prm.ws, WS_END);
  const rsrc_t rq = mk_rsrc(pwq + (size_t)(w * 56) * (H * DH * 2) + h * (DH * 2) + r * 64, 55 * (H * DH * 2) + 64);
  const rsrc_t rx = mk_rsrc(px + w * 112, DM * 2 + 112);
  const rsrc_t ro = mk_rsrc(pwo + (size_t)h * DH * (DM * 2) + w * 128, w < 7 ? 255 * (DM * 2) + 128 : 0);

  unsigned tag;
  v4u ring[WIN];
  v4u wqv[4], qg, ml;
  v2u x0, x1;
  v4u wb[32];
  v8u bt[8][2];

  auto q_share = [&]() __attribute__((always_inline)) {
    float a0[8], a1[8];
#pragma unroll
    for (int j = 0; j < 4; ++j) {
      const unsigned p0 = __builtin_amdgcn_perm(wqv[1][j], wqv[0][j], 0x05040100u), p1 = __builtin_amdgcn_perm(wqv[1][j], wqv[0][j], 0x07060302u);
      const unsigned p2 = __builtin_amdgcn_perm(wqv[3][j], wqv[2][j], 0x05040100u), p3 = __builtin_amdgcn_perm(wqv[3][j], wqv[2][j], 0x07060302u);
      a0[2 * j] = dot2(p2, x0[1], dot2(p0, x0[0], 0.f));
      a0[2 * j + 1] = dot2(p3, x0[1], dot2(p1, x0[0], 0.f));
      a1[2 * j] = dot2(p2, x1[1], dot2(p0, x1[0], 0.f));
      a1[2 * j + 1] = dot2(p3, x1[1], dot2(p1, x1[0], 0.f));
    }
    float z[8];
#pragma unroll
    for (int j = 0; j < 8; ++j) z[j] = swap32_add(a0[j], a1[j]);
    f32x4 u;
#pragma unroll
    for (int j = 0; j < 4; ++j) {
      float t = swap16_add(z[j], z[j + 4]);
      t += dppf<ROR4>(t);
      t += dppf<XOR8>(t);
      u[j] = t;
    }
    if ((lane & 12) == 0) *(f32x4*)(lds + L_QP + w * 64 + hh * 32 + (lane & 3) * 8 + ((lane >> 4) & 1) * 4) = u;
    ST(1);
    unsigned arrived = 0;
    asm volatile("s_waitcnt lgkmcnt(0)" : "+s"(tag)::"memory");
    if (lane == 0) arrived = __hip_atomic_fetch_add((unsigned*)lds + L_CNT, 1u, __ATOMIC_RELAXED, __HIP_MEMORY_SCOPE_WORKGROUP);
    asm volatile("" ::: "memory");
    if (__builtin_amdgcn_readfirstlane(arrived) == 7) {
      float q = 0.f;
#pragma unroll
      for (int i = 0; i < 8; ++i) q += lds[L_QP + i * 64 + lane];
      __builtin_amdgcn_raw_buffer_store_b32((pack_bf16(q, q) & 0xffffu) | (((lane & 1) ? tag : tag << 16) & 0xffff0000u), rw,
                                            WS_Q + (team * 2 + hh) * 1024 + ((lane >> 2) & 1) * 512 + (r * 4 + (l32 >> 3)) * 16 + (lane & 3) * 4, 0, AUX_SC1);
    }
    SB();
  };

  auto issue = LAMBDA(nc) {
    constexpr int n = decltype(nc)::value;
    if constexpr (n == N_WQ) {
      if (tid < 2) ((unsigned*)lds)[L_CNT + tid] = 0u;
      LDS_BARRIER();
      tag = *(const CAS unsigned*)((const CAS char*)prm.ws + WS_EPOCH + blk * 256) + 0x01010101u;
      const unsigned xoff = lane < 56 ? (lane >> 2) * 8 : OOB;
      x0 = __builtin_amdgcn_raw_buffer_load_b64(rx, xoff, 0, 0);
      x1 = __builtin_amdgcn_raw_buffer_load_b64(rx, xoff, DM * 2, 0);
#pragma unroll
      for (int i = 0; i < 4; ++i) wqv[i] = bld<0>(rq, (lane >> 2) * 4 * (H * DH * 2) + (lane & 3) * 16, i * (H * DH * 2));
    }
    if constexpr (n == LTOUCH) {
      if (w == 7 && s < 3) {
        const char* tp = s < 2 ? pv + kvoff + s * (SK * DH * 2 - 64) : (const char*)prm.out;
        const unsigned tv = *(const CAS unsigned*)tp;
        asm volatile("" ::"s"(tv));
      }
      SB();
    }
    if constexpr (n == N_QCALC) q_share();
    if constexpr (n >= FREE_N) throttle<n>(t_start);
    if constexpr (n == N_QPOLL) qg = bld<AUX_SC1>(rw, WS_Q + (team * 2 + b) * 1024 + l16, 0);
    if constexpr (n == N_ML) ml = bld<AUX_SC1>(rw, WS_ML + (grp * 16 + h) * 512 + l32 * 16, 0);
    SB();
    if constexpr ((n & 1) == 0) {
      asm volatile("" : "+s"(kvb));
      kvo = (kvb + ((n & 2) << 14) + ((n & 60) << 15)) & (SK * DH * 2 - 1);
    }
    ring[n % WIN] = bld<AUX_NT>(n < NLD ? rk : rv, l32 * 16 + hh * 2048 + (n & 1) * 1024, kvo);
    SB();
    static_for<0, 32>(LAMBDA(jc) {
      constexpr int j = decltype(jc)::value;
      if constexpr (n == (j < 4 ? WM0 + j * WMS : WO0 + 3 * j)) {
        wb[j] = bld<0>(ro, 32 * rc * (DM * 2) + c8 * 16 + (j & 3) * (DM * 2), (unsigned)(((j >> 2) ^ r) * 4 * (DM * 2)));
        SB();
      }
    });
    constexpr int P0 = WO0 + DIST + 1;
    static_assert(P0 + 31 * 3 < 2 * NLD, "all wo rows are paired inside the stream");
    if constexpr (n >= P0 && (n - P0) % 3 == 0 && (n - P0) / 3 < 32 && (((n - P0) / 3) & 1)) {
      constexpr int j = (n - P0) / 3, t = j >> 1;
#pragma unroll
      for (int jj = 0; jj < 4; ++jj) {
        unsigned lo = __builtin_amdgcn_perm(wb[2 * t + 1][jj], wb[2 * t][jj], 0x05040100u);
        unsigned hi = __builtin_amdgcn_perm(wb[2 * t + 1][jj], wb[2 * t][jj], 0x07060302u);
        asm volatile("" : "+v"(lo), "+v"(hi));
        bt[2 * jj][t >> 3][t & 7] = lo;
        bt[2 * jj + 1][t >> 3][t & 7] = hi;
      }
      if constexpr (j == 31) {
#pragma unroll
        for (int jj = 0; jj < 8; ++jj) asm volatile("" : "+v"(bt[jj][0]), "+v"(bt[jj][1]));
      }
      SB();
    }
  };
  auto after = LAMBDA(gc) {
    constexpr int n = issue_after(decltype(gc)::value);
    SB();
    if constexpr (n >= 0) issue(std::integral_constant<int, n>{});
  };

  static_for<0, WIN>(issue);
  if (__builtin_expect(!__all(tagged16(qg, tag)), 0)) {
    for (unsigned spins = 0; spins < SPIN_CAP; ++spins) {
      asm volatile("" ::: "memory");
      qg = bld<AUX_SC1>(rw, WS_Q + (team * 2 + b) * 1024 + l16, 0);
      if (__all(tagged16(qg, tag))) break;
    }
  }
  v4u qv;
  {
    const unsigned q0 = __builtin_amdgcn_perm(qg[1], qg[0], 0x05040100u), q1 = __builtin_amdgcn_perm(qg[3], qg[2], 0x05040100u);
    auto e = __builtin_amdgcn_permlane32_swap(q0, q0, false, false);
    auto o = __builtin_amdgcn_permlane32_swap(q1, q1, false, false);
    qv = v4u{e[0], o[0], e[1], o[1]};
  }
  ST(2);

  float sc[2];
  {
    float xs[16];
    float red[2];
    static_for<0, NLD>(LAMBDA(tc) {
      constexpr int t = decltype(tc)::value;
      const v4u cur = ring[t % WIN];
      xs[t & 15] = dot2(cur[3], qv[3], dot2(cur[2], qv[2], dot2(cur[1], qv[1], dot2(cur[0], qv[0], 0.f))));
      after(tc);
      if constexpr ((t & 15) == 15) {
#define TR_STEP(HALF, CTRL)                                               \
  _Pragma("unroll") for (int u = 0; u < (HALF); ++u) {                    \
    const float lo_ = xs[u], hi_ = xs[u + (HALF)];                        \
    const float t1_ = lo_ + dppf<CTRL>(lo_), t2_ = hi_ + dppf<CTRL>(hi_); \
    xs[u] = (lane & (HALF)) ? t2_ : t1_;                                  \
  }
        TR_STEP(8, XOR8) TR_STEP(4, XOR7) TR_STEP(2, XOR2) TR_STEP(1, XOR1)
#undef TR_STEP
        asm volatile("" : "+v"(xs[0]));
        red[(t >> 4) & 1] = xs[0];
        if constexpr ((t & 31) == 31) sc[t >> 5] = swap16_add(red[0], red[1]);
        SB();
      }
    });
  }
  ST(3);
  constexpr float C2 = 0.0625f * 1.4426950408889634f;
  const float mw = swap32_max(half_max(fmaxf(sc[0], sc[1]))) * C2;
  {
    const float p0 = __builtin_amdgcn_exp2f(fmaf(sc[0], C2, -mw));
    const float p1 = __builtin_amdgcn_exp2f(fmaf(sc[1], C2, -mw));
    lds[L_P + w * 128 + hh * 64 + l32] = p0;
    lds[L_P + w * 128 + hh * 64 + 32 + l32] = p1;
    float lw = half_sum(p0 + p1);
    lw = swap32_add(lw, lw);
    unsigned arrived = 0;
    if (lane == 0) {
      *(f32x2*)(lds + L_ML + w * 2) = f32x2{mw, lw};
      asm volatile("" ::: "memory");
      arrived = __hip_atomic_fetch_add((unsigned*)lds + L_CNT + 1, 1u, __ATOMIC_RELAXED, __HIP_MEMORY_SCOPE_WORKGROUP);
    }
    asm volatile("" ::: "memory");
    if (__builtin_amdgcn_readfirstlane(arrived) == 7) {
      const v2u e = *(const v2u*)(lds + L_ML + c8 * 2);
      __builtin_amdgcn_raw_buffer_store_b128(v4u{__builtin_amdgcn_perm(tag, e[0], 0x05040100u), __builtin_amdgcn_perm(tag, e[0], 0x07060302u),
                                                 __builtin_amdgcn_perm(tag, e[1], 0x05040100u), __builtin_amdgcn_perm(tag, e[1], 0x07060302u)}, rw,
                                             (lane < 8 ? WS_ML : OOB) + (grp * 16 + h) * 512 + (s * 8 + c8) * 16, 0, AUX_SC1);
    }
  }
  unsigned yst = (w < 7 ? WS_Y : OOB) + ((grp * 14 + w * 2 + hh) * 64 + me) * 128 + l32 * 4;
  unsigned ytag = (tag >> ((lane & 3) * 8)) & 255u;
  unsigned accrd = L_ACC * 4 + rc * 1024 + w * 128 + c8 * 16;
  const int kk = c8 ^ r;
  unsigned owr = L_O * 4 + (((kk >> 2) * 4 + ((kk >> 1) & 1) * 2 + (w >> 2)) * 16 + hh * 8 + (w & 1) * 4 + ((w >> 1) & 1) * 2 + (kk & 1)) * 4;
  asm volatile("s_waitcnt lgkmcnt(0)" : "+v"(yst), "+v"(ytag), "+v"(accrd), "+v"(owr)::"memory");
  SB();

  f32x2 acc[4];
#pragma unroll
  for (int j = 0; j < 4; ++j) acc[j] = f32x2{0.f, 0.f};
  float fw;
  bool mlok;
  auto ml_eval = [&]() __attribute__((always_inline)) {
    mlok = __all(tagged16(ml, tag));
    const float m = __uint_as_float(__builtin_amdgcn_perm(ml[1], ml[0], 0x05040100u));
    const float M = half_max(m);
    const float L = half_sum(__builtin_amdgcn_exp2f(m - M) * __uint_as_float(__builtin_amdgcn_perm(ml[3], ml[2], 0x05040100u)));
    fw = __builtin_amdgcn_exp2f(mw - M) / L;
  };
  {
    const f32x4* pl = (const f32x4*)(lds + L_P + w * 128 + hh * 64);
    f32x4 pc = pl[0];
    static_for<0, NLD>(LAMBDA(tc) {
      constexpr int t = decltype(tc)::value;
      const v4u cur = ring[(t + NLD) % WIN];
      const float pv_ = pc[t & 3];
      if constexpr ((t & 3) == 3 && t + 1 < NLD) pc = pl[(t + 1) >> 2];
#pragma unroll
      for (int j = 0; j < 4; ++j) {
        const f32x2 vv = {__uint_as_float(cur[j] << 16), __uint_as_float(cur[j] & 0xffff0000u)};
        acc[j] = __builtin_elementwise_fma(vv, f32x2{pv_, pv_}, acc[j]);
      }
      asm volatile("" : "+v"(acc[0]), "+v"(acc[1]), "+v"(acc[2]), "+v"(acc[3]));
      after(std::integral_constant<int, t + NLD>{});
      if constexpr (t + NLD == G_ML) {
        ml_eval();
        asm volatile("" : "+v"(fw));
        SB();
      }
    });
  }
  if (__builtin_expect(!mlok, 0)) {
    for (unsigned spins = 0; !mlok && spins < SPIN_CAP; ++spins) {
      __builtin_amdgcn_s_sleep(8);
      asm volatile("" ::: "memory");
      ml = bld<AUX_SC1>(rw, WS_ML + (grp * 16 + h) * 512 + l32 * 16, 0);
      ml_eval();
    }
  }
  ST(4);

  {
    f32x4 rr;
#pragma unroll
    for (int j = 0; j < 4; ++j) rr[j] = swap32_add(acc[j >> 1][j & 1], acc[2 + (j >> 1)][j & 1]) * fw;
    *(f32x4*)(lds + L_ACC + w * 256 + l32 * 8 + hh * 4) = rr;
  }
  LDS_BARRIER();
  const f32x4 a = *(const f32x4*)((const char*)lds + accrd);
  SB();
  int sidx = (lane & 2) ? (int)0xEEEEEEEEu : 0x44444444;
  unsigned ord = L_O * 4 + (lane >> 4) * 64 + (lane & 3) * 16;
  f32x4 dd[8];
#pragma unroll
  for (int j = 0; j < 8; ++j) {
    dd[j] = f32x4{0.f, 0.f, 0.f, 0.f};
    asm volatile("" : "+v"(dd[j]));
  }
  asm volatile("" : "+v"(sidx), "+v"(ord));
  SB();
  {
    float e0 = swap32_add(a[0], a[2]), e1 = swap32_add(a[1], a[3]);
    e0 += dppf<XOR8>(e0);
    e1 += dppf<XOR8>(e1);
    e0 = swap16_add(e0, e0);
    e1 = swap16_add(e1, e1);
    if ((lane & 24) == 0) *(unsigned*)((char*)lds + owr) = pack_bf16(e0, e1);
  }
  LDS_BARRIER();

  {
    const v4u oa0 = *(const v4u*)((const char*)lds + ord), oa1 = *(const v4u*)((const char*)lds + ord + 256);
#pragma unroll
    for (int j = 0; j < 8; ++j)
      dd[j] = __builtin_amdgcn_smfmac_f32_16x16x64_bf16(__builtin_bit_cast(bf16x8, oa0), __builtin_bit_cast(bf16x16, bt[j][0]), dd[j], sidx, 0, 0);
#pragma unroll
    for (int j = 0; j < 8; ++j)
      dd[j] = __builtin_amdgcn_smfmac_f32_16x16x64_bf16(__builtin_bit_cast(bf16x8, oa1), __builtin_bit_cast(bf16x16, bt[j][1]), dd[j], sidx, 0, 0);
    SB();
    float y[8];
#pragma unroll
    for (int j = 0; j < 8; ++j) y[j] = sel(lane & 8, dd[j][1] + dd[j][3], dd[j][0] + dd[j][2]);
    float u[2];
#pragma unroll
    for (int j = 0; j < 2; ++j) u[j] = sel(hh, sel(lane & 16, y[6 + j], y[4 + j]), sel(lane & 16, y[2 + j], y[j]));
    const float t0 = u[0] + dppf<XOR8>(u[0]), t1 = u[1] + dppf<XOR8>(u[1]);
    const float yv = (lane & 8) ? t1 : t0;
    __builtin_amdgcn_raw_buffer_store_b32((__float_as_uint(yv) & ~255u) | ytag, rw, yst, 0, AUX_SC1);
    ST(5);
  }
  if (w < 7 || me >= 14) { ST_FLUSH(); return; }

  {
    const unsigned yo = WS_Y + (grp * 14 + me) * 8192 + l16;
    const v4u tg = {tag & 255u, (tag >> 8) & 255u, (tag >> 16) & 255u, tag >> 24};
    const int ls = 32 * (me & 1) + 4 * c8 + 2 * hh + ((lane >> 4) & 1);
    GAS unsigned short* optr = (GAS unsigned short*)prm.out + b * (2 * DM) + blk * DM + 64 * (me >> 1) + 8 * (ls & 7) + (ls >> 3);
    __builtin_amdgcn_s_sleep(16);
    f32x4 sum;
    for (unsigned spins = 0;; ++spins) {
      v4u g[8];
#pragma unroll
      for (int t = 0; t < 8; ++t) g[t] = bld<AUX_SC1>(rw, yo, t * 1024);
      unsigned bad = 0;
      sum = f32x4{0.f, 0.f, 0.f, 0.f};
#pragma unroll
      for (int t = 0; t < 8; ++t) {
        const v4u x = g[t] ^ tg;
        bad |= (x[0] | x[1]) | (x[2] | x[3]);
        sum += __builtin_bit_cast(f32x4, x);
      }
      if (__all((bad & 255u) == 0) || spins > SPIN_CAP) break;
      asm volatile("" ::: "memory");
    }
    ST(6);
    float rr = swap16_add(swap32_add(sum[0], sum[2]), swap32_add(sum[1], sum[3]));
    rr += dppf<XOR8>(rr);
    if ((lane & 8) == 0) __hip_atomic_store(optr, (unsigned short)(pack_bf16(rr, rr) & 0xffffu), RLX_AGENT);
    if (me == 0 && b == 0 && lane == 0) __hip_atomic_store((GAS unsigned*)((GAS char*)prm.ws + WS_EPOCH + blk * 256), tag, RLX_AGENT);
    {
      const int el = (int)((unsigned)__builtin_amdgcn_s_memrealtime() - t_start);
      const int tgt = 10000 + 5 * (el - 3800);
      while ((int)((unsigned)__builtin_amdgcn_s_memrealtime() - t_start) < tgt) __builtin_amdgcn_s_sleep(1);
    }
    ST(7);
    ST_FLUSH();
  }
}

extern "C" void kernel_launch(void* const* d_in, const int* in_sizes, int n_in, void* d_out, int out_size, void* d_ws,
                              size_t ws_size, hipStream_t stream) {
  Params p;
  p.x[0] = d_in[0];
  p.k[0] = d_in[1];
  p.v[0] = d_in[2];
  p.wq[0] = d_in[3];
  p.wo[0] = d_in[4];
  p.x[1] = d_in[5];
  p.k[1] = d_in[6];
  p.v[1] = d_in[7];
  p.wq[1] = d_in[8];
  p.wo[1] = d_in[9];
  p.out = d_out;
  p.ws = d_ws;
  mha2_decode_kernel<<<dim3(256), dim3(NTHR), 0, stream>>>(p);
}
```

```cpp
#define AMP
#include <hip/hip_runtime.h>
#include <hip/hip_bf16.h>
#include <stdint.h>
#include <type_traits>

#define GAS __attribute__((address_space(1)))
#define CAS __attribute__((address_space(4)))
typedef unsigned v2u __attribute__((ext_vector_type(2)));
typedef unsigned v4u __attribute__((ext_vector_type(4)));
typedef unsigned v8u __attribute__((ext_vector_type(8)));
typedef float f32x2 __attribute__((ext_vector_type(2)));
typedef float f32x4 __attribute__((ext_vector_type(4)));
typedef __bf16 bf16x2 __attribute__((ext_vector_type(2)));
typedef __bf16 bf16x8 __attribute__((ext_vector_type(8)));
typedef __bf16 bf16x16 __attribute__((ext_vector_type(16)));
#define RLX_AGENT __ATOMIC_RELAXED, __HIP_MEMORY_SCOPE_AGENT

constexpr int WIN = 16;
constexpr int DIST = 6;
constexpr int FREE_N = 4;
constexpr int N_WQ = 2;
constexpr int N_QCALC = 6;
constexpr int N_QPOLL = 13;
constexpr int WM0 = 6, WMS = 2;
constexpr int WO0 = 12;
constexpr int N_ML = 96;
constexpr int G_ML = 123;
constexpr int PACE_T0 = 30;
constexpr int PACE_DUR = 3560;
constexpr int PACE_GAIN = 128;

constexpr int H = 16, SK = 4096, DH = 256, DM = 448;
constexpr int NTHR = 512;
constexpr int NLD = 64;
constexpr unsigned WS_EPOCH = 0;
constexpr unsigned WS_Q = 4096;
constexpr unsigned WS_ML = WS_Q + 65536;
constexpr unsigned WS_Y = WS_ML + 32768;
constexpr unsigned WS_END = WS_Y + 4 * 14 * 8192;
constexpr unsigned SPIN_CAP = 1u << 20;
constexpr unsigned OOB = 0x7fffff00u;
constexpr int AUX_NT = 2, AUX_SC1 = 16;

struct Params {
  const void* k[2];
  const void* wq[2];
  const void* x[2];
  void* ws;
  void* out;
  const void* v[2];
  const void* wo[2];
};

__device__ __forceinline__ float dot2(unsigned a, unsigned b, float c) {
  return __builtin_amdgcn_fdot2_f32_bf16(__builtin_bit_cast(bf16x2, a), __builtin_bit_cast(bf16x2, b), c, false);
}
__device__ __forceinline__ unsigned pack_bf16(float lo, float hi) {
  bf16x2 r;
  r.x = (__bf16)lo;
  r.y = (__bf16)hi;
  return __builtin_bit_cast(unsigned, r);
}
template <int CTRL> __device__ __forceinline__ float dppf(float x) {
  return __builtin_bit_cast(float, __builtin_amdgcn_mov_dpp(__builtin_bit_cast(int, x), CTRL, 0xf, 0xf, true));
}
constexpr int XOR1 = 0xB1, XOR2 = 0x4E, XOR7 = 0x141, XOR8 = 0x128, ROR4 = 0x124;
__device__ __forceinline__ float swap16_add(float a, float b) {
  auto r = __builtin_amdgcn_permlane16_swap(__float_as_uint(a), __float_as_uint(b), false, false);
  return __uint_as_float(r[0]) + __uint_as_float(r[1]);
}
__device__ __forceinline__ float swap32_add(float a, float b) {
  auto r = __builtin_amdgcn_permlane32_swap(__float_as_uint(a), __float_as_uint(b), false, false);
  return __uint_as_float(r[0]) + __uint_as_float(r[1]);
}
__device__ __forceinline__ float swap32_max(float a) {
  auto r = __builtin_amdgcn_permlane32_swap(__float_as_uint(a), __float_as_uint(a), false, false);
  return fmaxf(__uint_as_float(r[0]), __uint_as_float(r[1]));
}
__device__ __forceinline__ float half_max(float v) {
  v = fmaxf(v, dppf<XOR1>(v));
  v = fmaxf(v, dppf<XOR2>(v));
  v = fmaxf(v, dppf<XOR7>(v));
  v = fmaxf(v, dppf<XOR8>(v));
  auto r = __builtin_amdgcn_permlane16_swap(__float_as_uint(v), __float_as_uint(v), false, false);
  return fmaxf(__uint_as_float(r[0]), __uint_as_float(r[1]));
}
__device__ __forceinline__ float half_sum(float v) {
  v += dppf<XOR1>(v);
  v += dppf<XOR2>(v);
  v += dppf<XOR7>(v);
  v += dppf<XOR8>(v);
  return swap16_add(v, v);
}
__device__ __forceinline__ float sel(bool c, float a, float b) {
  asm volatile("" : "+v"(a), "+v"(b));
  return c ? a : b;
}
__device__ __forceinline__ bool tagged16(v4u g, unsigned tag) {
  return __builtin_amdgcn_perm(g[1], g[0], 0x07060302u) == tag && __builtin_amdgcn_perm(g[3], g[2], 0x07060302u) == tag;
}
template <int I, int N, class F> __device__ __forceinline__ void static_for(F&& f) {
  if constexpr (I < N) {
    f(std::integral_constant<int, I>{});
    static_for<I + 1, N>(f);
  }
}
#define LAMBDA(v) [&](auto v) __attribute__((always_inline))
typedef __amdgpu_buffer_rsrc_t rsrc_t;
__device__ __forceinline__ rsrc_t mk_rsrc(const void* base, unsigned bytes) {
  return __builtin_amdgcn_make_buffer_rsrc((void*)base, 0, bytes, 0x00020000);
}
template <int AUX> __device__ __forceinline__ v4u bld(rsrc_t rs, unsigned voff, unsigned soff) {
  return __builtin_amdgcn_raw_buffer_load_b128(rs, voff, soff, AUX);
}
#define SB() __builtin_amdgcn_sched_barrier(0)
#define LDS_BARRIER()                                  \
  do {                                                 \
    asm volatile("s_waitcnt lgkmcnt(0)" ::: "memory"); \
    __builtin_amdgcn_s_barrier();                      \
    asm volatile("" ::: "memory");                     \
    SB();                                              \
  } while (0)

constexpr int pace_thr(int n, int depth) {
  const int dith16 = 2 * (((n & 1) << 2) | (n & 2) | ((n & 4) >> 2)) + 1;
  const int lag = depth == 2   ? -50
                  : depth <= 4 ? PACE_GAIN * (16 * (depth - 2) - dith16) / 16
                               : 2 * PACE_GAIN + (PACE_GAIN + 300 * n / 128) * (16 - dith16) / 16;
  return PACE_T0 + n * PACE_DUR / 128 + lag;
}
template <int N> __device__ __forceinline__ void throttle(unsigned t_start) {
  const int el = (int)((unsigned)__builtin_amdgcn_s_memrealtime() - t_start);
  asm volatile(
      "s_waitcnt vmcnt(5)\n\ts_cmpk_ge_i32 %0, %1\n\ts_cbranch_scc1 1f\n\ts_waitcnt vmcnt(3)\n\t"
      "s_cmpk_ge_i32 %0, %2\n\ts_cbranch_scc1 1f\n\ts_waitcnt vmcnt(2)\n\t"
      "s_cmpk_ge_i32 %0, %3\n\ts_cbranch_scc1 1f\n\ts_waitcnt vmcnt(1)\n\t"
      "s_cmpk_ge_i32 %0, %4\n\ts_cbranch_scc1 1f\n\ts_waitcnt vmcnt(0)\n1:" ::"s"(el),
      "i"(pace_thr(N, 5)), "i"(pace_thr(N, 4)), "i"(pace_thr(N, 3)), "i"(pace_thr(N, 2))
      : "scc");
}
template <int N> __device__ __forceinline__ void throttle_hold(unsigned t_start, int& lag, unsigned hold) {
  constexpr int D = PACE_T0 + N * PACE_DUR / 128;
  asm volatile(
      "s_cmp_lg_u32 %2, 0\n\ts_cbranch_scc1 2f\n\ts_memrealtime s[66:67]\n\ts_waitcnt lgkmcnt(0)\n\t"
      "s_sub_u32 %0, s66, %1\n\ts_sub_u32 %0, %0, %3\n2:\n\t"
      "s_waitcnt vmcnt(5)\n\ts_cmpk_ge_i32 %0, %4\n\ts_cbranch_scc1 1f\n\ts_waitcnt vmcnt(3)\n\t"
      "s_cmpk_ge_i32 %0, %5\n\ts_cbranch_scc1 1f\n\ts_waitcnt vmcnt(2)\n\t"
      "s_cmpk_ge_i32 %0, %6\n\ts_cbranch_scc1 1f\n\ts_waitcnt vmcnt(1)\n\t"
      "s_cmpk_ge_i32 %0, %7\n\ts_cbranch_scc1 1f\n\ts_waitcnt vmcnt(0)\n1:"
      : "+s"(lag)
      : "s"(t_start), "s"(hold), "i"(D), "i"(pace_thr(N, 5) - D), "i"(pace_thr(N, 4) - D), "i"(pace_thr(N, 3) - D), "i"(pace_thr(N, 2) - D)
      : "scc", "s66", "s67");
}
constexpr int issue_after(int g) {
  const int n = g < 2 * (WIN - DIST) ? ((g & 1) ? WIN + g / 2 : -1) : g + DIST;
  return n < 2 * NLD ? n : -1;
}

#define ST(k)
#define ST_FLUSH()

constexpr int L_QP = 0;
constexpr int L_P = L_QP + 8 * 64;
constexpr int L_ACC = L_P + 8 * 128;
constexpr int L_O = L_ACC + 8 * 256;
constexpr int L_ML = L_O + 128;
constexpr int L_CNT = L_ML + 16;
constexpr int L_END = L_CNT + 4;

__global__ void __launch_bounds__(NTHR, 2) mha2_decode_kernel(Params prm) {
  __shared__ __attribute__((aligned(16))) float lds[L_END];
  ST(0);
  const unsigned t_start = (unsigned)__builtin_amdgcn_s_memrealtime();
  const int tid = threadIdx.x, lane = tid & 63;
  const int w = __builtin_amdgcn_readfirstlane(tid >> 6);
  const int bid = blockIdx.x;
  const int team = (bid & 7) * 4 + (bid >> 6);
  const int blk = team >> 4, h = team & 15;
  const int r = (bid >> 3) & 7, b = r >> 2, s = r & 3;
  const int hh = lane >> 5, l32 = lane & 31, rc = lane >> 3, c8 = lane & 7;
  const int grp = blk * 2 + b, me = s * 16 + h;
  const char* const pk = (const char*)(blk ? prm.k[1] : prm.k[0]);
  const char* const pv = (const char*)(blk ? prm.v[1] : prm.v[0]);
  const char* const pwq = (const char*)(blk ? prm.wq[1] : prm.wq[0]);
  const char* const pwo = (const char*)(blk ? prm.wo[1] : prm.wo[0]);
  const char* const px = (const char*)(blk ? prm.x[1] : prm.x[0]);

  const size_t kvoff = (size_t)(b * H + h) * SK * DH * 2;
  const rsrc_t rk = mk_rsrc(pk + kvoff, SK * DH * 2);
  const rsrc_t rv = mk_rsrc(pv + kvoff, SK * DH * 2);
  unsigned kvb = (unsigned)(grp * 16 + h) * 32768u + ((w & 1) << 9) + ((w & 6) << 11) + ((s & 1) << 14) + ((s & 2) << 15);
  unsigned kvo = 0;
  const unsigned l16 = lane * 16;
  const rsrc_t rw = mk_rsrc(prm.ws, WS_END);
  const rsrc_t rq = mk_rsrc(pwq + (size_t)(w * 56) * (H * DH * 2) + h * (DH * 2) + r * 64, 55 * (H * DH * 2) + 64);
  const rsrc_t rx = mk_rsrc(px + w * 112, DM * 2 + 112);
  const rsrc_t ro = mk_rsrc(pwo + (size_t)h * DH * (DM * 2) + w * 128, w < 7 ? 255 * (DM * 2) + 128 : 0);

  unsigned tag;
  v4u ring[WIN];
  v4u wqv[4], qg, ml;
  v2u x0, x1;
  int lag = 0;
  unsigned tv;
  const unsigned hold = (unsigned)__builtin_amdgcn_readfirstlane(((w + 1) >> 3) & ~(s & (s >> 1)) & 1);
  v4u wb[32];
  v8u bt[8][2];

  auto q_share = [&]() __attribute__((always_inline)) {
    float a0[8], a1[8];
#pragma unroll
    for (int j = 0; j < 4; ++j) {
      const unsigned p0 = __builtin_amdgcn_perm(wqv[1][j], wqv[0][j], 0x05040100u), p1 = __builtin_amdgcn_perm(wqv[1][j], wqv[0][j], 0x07060302u);
      const unsigned p2 = __builtin_amdgcn_perm(wqv[3][j], wqv[2][j], 0x05040100u), p3 = __builtin_amdgcn_perm(wqv[3][j], wqv[2][j], 0x07060302u);
      a0[2 * j] = dot2(p2, x0[1], dot2(p0, x0[0], 0.f));
      a0[2 * j + 1] = dot2(p3, x0[1], dot2(p1, x0[0], 0.f));
      a1[2 * j] = dot2(p2, x1[1], dot2(p0, x1[0], 0.f));
      a1[2 * j + 1] = dot2(p3, x1[1], dot2(p1, x1[0], 0.f));
    }
    float z[8];
#pragma unroll
    for (int j = 0; j < 8; ++j) z[j] = swap32_add(a0[j], a1[j]);
    f32x4 u;
#pragma unroll
    for (int j = 0; j < 4; ++j) {
      float t = swap16_add(z[j], z[j + 4]);
      t += dppf<ROR4>(t);
      t += dppf<XOR8>(t);
      u[j] = t;
    }
    if ((lane & 12) == 0) *(f32x4*)(lds + L_QP + w * 64 + hh * 32 + (lane & 3) * 8 + ((lane >> 4) & 1) * 4) = u;
    ST(1);
    unsigned arrived = 0;
    asm volatile("s_waitcnt lgkmcnt(0)" : "+s"(tag)::"memory");
    if (lane == 0) arrived = __hip_atomic_fetch_add((unsigned*)lds + L_CNT, 1u, __ATOMIC_RELAXED, __HIP_MEMORY_SCOPE_WORKGROUP);
    asm volatile("" ::: "memory");
    if (__builtin_amdgcn_readfirstlane(arrived) == 7) {
      float q = 0.f;
#pragma unroll
      for (int i = 0; i < 8; ++i) q += lds[L_QP + i * 64 + lane];
      __builtin_amdgcn_raw_buffer_store_b32((pack_bf16(q, q) & 0xffffu) | (((lane & 1) ? tag : tag << 16) & 0xffff0000u), rw,
                                            WS_Q + (team * 2 + hh) * 1024 + ((lane >> 2) & 1) * 512 + (r * 4 + (l32 >> 3)) * 16 + (lane & 3) * 4, 0, AUX_SC1);
    }
    SB();
  };

  auto issue = LAMBDA(nc) {
    constexpr int n = decltype(nc)::value;
    if constexpr (n == N_WQ) {
      if (tid < 2) ((unsigned*)lds)[L_CNT + tid] = 0u;
      LDS_BARRIER();
      tag = *(const CAS unsigned*)((const CAS char*)prm.ws + WS_EPOCH + blk * 256) + 0x01010101u;
      const unsigned xoff = lane < 56 ? (lane >> 2) * 8 : OOB;
      x0 = __builtin_amdgcn_raw_buffer_load_b64(rx, xoff, 0, 0);
      x1 = __builtin_amdgcn_raw_buffer_load_b64(rx, xoff, DM * 2, 0);
#pragma unroll
      for (int i = 0; i < 4; ++i) wqv[i] = bld<0>(rq, (lane >> 2) * 4 * (H * DH * 2) + (lane & 3) * 16, i * (H * DH * 2));
    }
    if constexpr (n == N_QCALC) q_share();
    if constexpr (n >= FREE_N) throttle<n>(t_start);
    if constexpr (n == N_QPOLL) qg = bld<AUX_SC1>(rw, WS_Q + (team * 2 + b) * 1024 + l16, 0);
    if constexpr (n == N_ML) ml = bld<AUX_SC1>(rw, WS_ML + (grp * 16 + h) * 512 + l32 * 16, 0);
    SB();
    if constexpr ((n & 1) == 0) {
      asm volatile("" : "+s"(kvb));
      kvo = (kvb + ((n & 2) << 14) + ((n & 60) << 15)) & (SK * DH * 2 - 1);
    }
    ring[n % WIN] = bld<AUX_NT>(n < NLD ? rk : rv, l32 * 16 + hh * 2048 + (n & 1) * 1024, kvo);
    SB();
    static_for<0, 32>(LAMBDA(jc) {
      constexpr int j = decltype(jc)::value;
      if constexpr (n == (j < 4 ? WM0 + j * WMS : WO0 + 3 * j)) {
        wb[j] = bld<0>(ro, 32 * rc * (DM * 2) + c8 * 16 + (j & 3) * (DM * 2), (unsigned)(((j >> 2) ^ r) * 4 * (DM * 2)));
        SB();
      }
    });
    constexpr int P0 = WO0 + DIST + 1;
    static_assert(P0 + 31 * 3 < 2 * NLD, "all wo rows are paired inside the stream");
    if constexpr (n >= P0 && (n - P0) % 3 == 0 && (n - P0) / 3 < 32 && (((n - P0) / 3) & 1)) {
      constexpr int j = (n - P0) / 3, t = j >> 1;
#pragma unroll
      for (int jj = 0; jj < 4; ++jj) {
        unsigned lo = __builtin_amdgcn_perm(wb[2 * t + 1][jj], wb[2 * t][jj], 0x05040100u);
        unsigned hi = __builtin_amdgcn_perm(wb[2 * t + 1][jj], wb[2 * t][jj], 0x07060302u);
        asm volatile("" : "+v"(lo), "+v"(hi));
        bt[2 * jj][t >> 3][t & 7] = lo;
        bt[2 * jj + 1][t >> 3][t & 7] = hi;
      }
      if constexpr (j == 31) {
#pragma unroll
        for (int jj = 0; jj < 8; ++jj) asm volatile("" : "+v"(bt[jj][0]), "+v"(bt[jj][1]));
      }
      SB();
    }
  };
  auto after = LAMBDA(gc) {
    constexpr int n = issue_after(decltype(gc)::value);
    SB();
    if constexpr (n >= 0) issue(std::integral_constant<int, n>{});
  };

  static_for<0, WIN>(issue);
  if (__builtin_expect(!__all(tagged16(qg, tag)), 0)) {
    for (unsigned spins = 0; spins < SPIN_CAP; ++spins) {
      asm volatile("" ::: "memory");
      qg = bld<AUX_SC1>(rw, WS_Q + (team * 2 + b) * 1024 + l16, 0);
      if (__all(tagged16(qg, tag))) break;
    }
  }
  v4u qv;
  {
    const unsigned q0 = __builtin_amdgcn_perm(qg[1], qg[0], 0x05040100u), q1 = __builtin_amdgcn_perm(qg[3], qg[2], 0x05040100u);
    auto e = __builtin_amdgcn_permlane32_swap(q0, q0, false, false);
    auto o = __builtin_amdgcn_permlane32_swap(q1, q1, false, false);
    qv = v4u{e[0], o[0], e[1], o[1]};
  }
  ST(2);

  float sc[2];
  {
    float xs[16];
    float red[2];
    static_for<0, NLD>(LAMBDA(tc) {
      constexpr int t = decltype(tc)::value;
      const v4u cur = ring[t % WIN];
      xs[t & 15] = dot2(cur[3], qv[3], dot2(cur[2], qv[2], dot2(cur[1], qv[1], dot2(cur[0], qv[0], 0.f))));
      asm volatile("" : "+v"(xs[t & 15]));
      after(tc);
      if constexpr ((t & 15) == 15) {
#define TR_STEP(HALF, CTRL)                                               \
  _Pragma("unroll") for (int u = 0; u < (HALF); ++u) {                    \
    const float lo_ = xs[u], hi_ = xs[u + (HALF)];                        \
    const float t1_ = lo_ + dppf<CTRL>(lo_), t2_ = hi_ + dppf<CTRL>(hi_); \
    xs[u] = (lane & (HALF)) ? t2_ : t1_;                                  \
  }
        TR_STEP(8, XOR8) TR_STEP(4, XOR7) TR_STEP(2, XOR2) TR_STEP(1, XOR1)
#undef TR_STEP
        asm volatile("" : "+v"(xs[0]));
        red[(t >> 4) & 1] = xs[0];
        if constexpr ((t & 31) == 31) sc[t >> 5] = swap16_add(red[0], red[1]);
        SB();
      }
    });
  }
  ST(3);
  constexpr float C2 = 0.0625f * 1.4426950408889634f;
  const float mw = swap32_max(half_max(fmaxf(sc[0], sc[1]))) * C2;
  {
    const float p0 = __builtin_amdgcn_exp2f(fmaf(sc[0], C2, -mw));
    const float p1 = __builtin_amdgcn_exp2f(fmaf(sc[1], C2, -mw));
    lds[L_P + w * 128 + hh * 64 + l32] = p0;
    lds[L_P + w * 128 + hh * 64 + 32 + l32] = p1;
    float lw = half_sum(p0 + p1);
    lw = swap32_add(lw, lw);
    unsigned arrived = 0;
    if (lane == 0) {
      *(f32x2*)(lds + L_ML + w * 2) = f32x2{mw, lw};
      asm volatile("" ::: "memory");
      arrived = __hip_atomic_fetch_add((unsigned*)lds + L_CNT + 1, 1u, __ATOMIC_RELAXED, __HIP_MEMORY_SCOPE_WORKGROUP);
    }
    asm volatile("" ::: "memory");
    if (__builtin_amdgcn_readfirstlane(arrived) == 7) {
      const v2u e = *(const v2u*)(lds + L_ML + c8 * 2);
      __builtin_amdgcn_raw_buffer_store_b128(v4u{__builtin_amdgcn_perm(tag, e[0], 0x05040100u), __builtin_amdgcn_perm(tag, e[0], 0x07060302u),
                                                 __builtin_amdgcn_perm(tag, e[1], 0x05040100u), __builtin_amdgcn_perm(tag, e[1], 0x07060302u)}, rw,
                                             (lane < 8 ? WS_ML : OOB) + (grp * 16 + h) * 512 + (s * 8 + c8) * 16, 0, AUX_SC1);
    }
  }
  unsigned yst = (w < 7 ? WS_Y : OOB) + ((grp * 14 + w * 2 + hh) * 64 + me) * 128 + l32 * 4;
  unsigned ytag = (tag >> ((lane & 3) * 8)) & 255u;
  unsigned accrd = L_ACC * 4 + rc * 1024 + w * 128 + c8 * 16;
  const int kk = c8 ^ r;
  unsigned owr = L_O * 4 + (((kk >> 2) * 4 + ((kk >> 1) & 1) * 2 + (w >> 2)) * 16 + hh * 8 + (w & 1) * 4 + ((w >> 1) & 1) * 2 + (kk & 1)) * 4;
  asm volatile("s_waitcnt lgkmcnt(0)" : "+v"(yst), "+v"(ytag), "+v"(accrd), "+v"(owr)::"memory");
  SB();

  f32x2 acc[4];
#pragma unroll
  for (int j = 0; j < 4; ++j) acc[j] = f32x2{0.f, 0.f};
  float fw;
  bool mlok;
  auto ml_eval = [&]() __attribute__((always_inline)) {
    mlok = __all(tagged16(ml, tag));
    const float m = __uint_as_float(__builtin_amdgcn_perm(ml[1], ml[0], 0x05040100u));
    const float M = half_max(m);
    const float L = half_sum(__builtin_amdgcn_exp2f(m - M) * __uint_as_float(__builtin_amdgcn_perm(ml[3], ml[2], 0x05040100u)));
    fw = __builtin_amdgcn_exp2f(mw - M) / L;
  };
  {
    const f32x4* pl = (const f32x4*)(lds + L_P + w * 128 + hh * 64);
    f32x4 pc = pl[0];
    static_for<0, NLD>(LAMBDA(tc) {
      constexpr int t = decltype(tc)::value;
      const v4u cur = ring[(t + NLD) % WIN];
      const float pv_ = pc[t & 3];
      if constexpr ((t & 3) == 3 && t + 1 < NLD) pc = pl[(t + 1) >> 2];
#pragma unroll
      for (int j = 0; j < 4; ++j) {
        const f32x2 vv = {__uint_as_float(cur[j] << 16), __uint_as_float(cur[j] & 0xffff0000u)};
        acc[j] = __builtin_elementwise_fma(vv, f32x2{pv_, pv_}, acc[j]);
      }
      asm volatile("" : "+v"(acc[0]), "+v"(acc[1]), "+v"(acc[2]), "+v"(acc[3]));
      after(std::integral_constant<int, t + NLD>{});
      if constexpr (t + NLD == G_ML) {
        ml_eval();
        asm volatile("" : "+v"(fw));
        SB();
      }
    });
  }
  if (__builtin_expect(!mlok, 0)) {
    for (unsigned spins = 0; !mlok && spins < SPIN_CAP; ++spins) {
      __builtin_amdgcn_s_sleep(8);
      asm volatile("" ::: "memory");
      ml = bld<AUX_SC1>(rw, WS_ML + (grp * 16 + h) * 512 + l32 * 16, 0);
      ml_eval();
    }
  }
  ST(4);

  {
    f32x4 rr;
#pragma unroll
    for (int j = 0; j < 4; ++j) rr[j] = swap32_add(acc[j >> 1][j & 1], acc[2 + (j >> 1)][j & 1]) * fw;
    *(f32x4*)(lds + L_ACC + w * 256 + l32 * 8 + hh * 4) = rr;
  }
  LDS_BARRIER();
  const f32x4 a = *(const f32x4*)((const char*)lds + accrd);
  SB();
  int sidx = (lane & 2) ? (int)0xEEEEEEEEu : 0x44444444;
  unsigned ord = L_O * 4 + (lane >> 4) * 64 + (lane & 3) * 16;
  f32x4 dd[8];
#pragma unroll
  for (int j = 0; j < 8; ++j) {
    dd[j] = f32x4{0.f, 0.f, 0.f, 0.f};
    asm volatile("" : "+v"(dd[j]));
  }
  asm volatile("" : "+v"(sidx), "+v"(ord));
  SB();
  {
    float e0 = swap32_add(a[0], a[2]), e1 = swap32_add(a[1], a[3]);
    e0 += dppf<XOR8>(e0);
    e1 += dppf<XOR8>(e1);
    e0 = swap16_add(e0, e0);
    e1 = swap16_add(e1, e1);
    if ((lane & 24) == 0) *(unsigned*)((char*)lds + owr) = pack_bf16(e0, e1);
  }
  LDS_BARRIER();

  {
    const v4u oa0 = *(const v4u*)((const char*)lds + ord), oa1 = *(const v4u*)((const char*)lds + ord + 256);
#pragma unroll
    for (int j = 0; j < 8; ++j)
      dd[j] = __builtin_amdgcn_smfmac_f32_16x16x64_bf16(__builtin_bit_cast(bf16x8, oa0), __builtin_bit_cast(bf16x16, bt[j][0]), dd[j], sidx, 0, 0);
#pragma unroll
    for (int j = 0; j < 8; ++j)
      dd[j] = __builtin_amdgcn_smfmac_f32_16x16x64_bf16(__builtin_bit_cast(bf16x8, oa1), __builtin_bit_cast(bf16x16, bt[j][1]), dd[j], sidx, 0, 0);
    SB();
    float y[8];
#pragma unroll
    for (int j = 0; j < 8; ++j) y[j] = sel(lane & 8, dd[j][1] + dd[j][3], dd[j][0] + dd[j][2]);
    float u[2];
#pragma unroll
    for (int j = 0; j < 2; ++j) u[j] = sel(hh, sel(lane & 16, y[6 + j], y[4 + j]), sel(lane & 16, y[2 + j], y[j]));
    const float t0 = u[0] + dppf<XOR8>(u[0]), t1 = u[1] + dppf<XOR8>(u[1]);
    const float yv = (lane & 8) ? t1 : t0;
    __builtin_amdgcn_raw_buffer_store_b32((__float_as_uint(yv) & ~255u) | ytag, rw, yst, 0, AUX_SC1);
    ST(5);
  }
  if (w < 7 || me >= 14) { ST_FLUSH(); return; }

  {
    const unsigned yo = WS_Y + (grp * 14 + me) * 8192 + l16;
    const v4u tg = {tag & 255u, (tag >> 8) & 255u, (tag >> 16) & 255u, tag >> 24};
    const int ls = 32 * (me & 1) + 4 * c8 + 2 * hh + ((lane >> 4) & 1);
    GAS unsigned short* optr = (GAS unsigned short*)prm.out + b * (2 * DM) + blk * DM + 64 * (me >> 1) + 8 * (ls & 7) + (ls >> 3);
    __builtin_amdgcn_s_sleep(16);
    f32x4 sum;
    for (unsigned spins = 0;; ++spins) {
      v4u g[8];
#pragma unroll
      for (int t = 0; t < 8; ++t) g[t] = bld<AUX_SC1>(rw, yo, t * 1024);
      unsigned bad = 0;
      sum = f32x4{0.f, 0.f, 0.f, 0.f};
#pragma unroll
      for (int t = 0; t < 8; ++t) {
        const v4u x = g[t] ^ tg;
        bad |= (x[0] | x[1]) | (x[2] | x[3]);
        sum += __builtin_bit_cast(f32x4, x);
      }
      if (__all((bad & 255u) == 0) || spins > SPIN_CAP) break;
      asm volatile("" ::: "memory");
    }
    ST(6);
    float rr = swap16_add(swap32_add(sum[0], sum[2]), swap32_add(sum[1], sum[3]));
    rr += dppf<XOR8>(rr);
    if ((lane & 8) == 0) __hip_atomic_store(optr, (unsigned short)(pack_bf16(rr, rr) & 0xffffu), RLX_AGENT);
    if (me == 0 && b == 0 && lane == 0) __hip_atomic_store((GAS unsigned*)((GAS char*)prm.ws + WS_EPOCH + blk * 256), tag, RLX_AGENT);
    {
      const int el = (int)((unsigned)__builtin_amdgcn_s_memrealtime() - t_start);
      const int tgt = 10000 + 5 * (el - 3800);
      while ((int)((unsigned)__builtin_amdgcn_s_memrealtime() - t_start) < tgt) __builtin_amdgcn_s_sleep(1);
    }
    ST(7);
    ST_FLUSH();
  }
}

extern "C" void kernel_launch(void* const* d_in, const int* in_sizes, int n_in, void* d_out, int out_size, void* d_ws,
                              size_t ws_size, hipStream_t stream) {
  Params p;
  p.x[0] = d_in[0];
  p.k[0] = d_in[1];
  p.v[0] = d_in[2];
  p.wq[0] = d_in[3];
  p.wo[0] = d_in[4];
  p.x[1] = d_in[5];
  p.k[1] = d_in[6];
  p.v[1] = d_in[7];
  p.wq[1] = d_in[8];
  p.wo[1] = d_in[9];
  p.out = d_out;
  p.ws = d_ws;
  mha2_decode_kernel<<<dim3(256), dim3(NTHR), 0, stream>>>(p);
}
```

```cpp
#define AMP
#define AMPK 1
#include <hip/hip_runtime.h>
#include <hip/hip_bf16.h>
#include <stdint.h>
#include <type_traits>

#define GAS __attribute__((address_space(1)))
#define CAS __attribute__((address_space(4)))
typedef unsigned v2u __attribute__((ext_vector_type(2)));
typedef unsigned v4u __attribute__((ext_vector_type(4)));
typedef unsigned v8u __attribute__((ext_vector_type(8)));
typedef float f32x2 __attribute__((ext_vector_type(2)));
typedef float f32x4 __attribute__((ext_vector_type(4)));
typedef __bf16 bf16x2 __attribute__((ext_vector_type(2)));
typedef __bf16 bf16x8 __attribute__((ext_vector_type(8)));
typedef __bf16 bf16x16 __attribute__((ext_vector_type(16)));
#define RLX_AGENT __ATOMIC_RELAXED, __HIP_MEMORY_SCOPE_AGENT

constexpr int WIN = 16;
constexpr int DIST = 6;
constexpr int FREE_N = 4;
constexpr int N_WQ = 2;
constexpr int N_QCALC = 6;
constexpr int N_QPOLL = 13;
constexpr int WM0 = 6, WMS = 2;
constexpr int WO0 = 12;
constexpr int N_ML = 96;
constexpr int G_ML = 123;
constexpr int PACE_T0 = 30;
constexpr int PACE_DUR = 3560;
constexpr int PACE_GAIN = 128;

constexpr int H = 16, SK = 4096, DH = 256, DM = 448;
constexpr int NTHR = 512;
constexpr int NLD = 64;
constexpr unsigned WS_EPOCH = 0;
constexpr unsigned WS_Q = 4096;
constexpr unsigned WS_ML = WS_Q + 65536;
constexpr unsigned WS_Y = WS_ML + 32768;
constexpr unsigned WS_END = WS_Y + 4 * 14 * 8192;
constexpr unsigned SPIN_CAP = 1u << 20;
constexpr unsigned OOB = 0x7fffff00u;
constexpr int AUX_NT = 2, AUX_SC1 = 16;

struct Params {
  const void* k[2];
  const void* wq[2];
  const void* x[2];
  void* ws;
  void* out;
  const void* v[2];
  const void* wo[2];
};

__device__ __forceinline__ float dot2(unsigned a, unsigned b, float c) {
  return __builtin_amdgcn_fdot2_f32_bf16(__builtin_bit_cast(bf16x2, a), __builtin_bit_cast(bf16x2, b), c, false);
}
__device__ __forceinline__ unsigned pack_bf16(float lo, float hi) {
  bf16x2 r;
  r.x = (__bf16)lo;
  r.y = (__bf16)hi;
  return __builtin_bit_cast(unsigned, r);
}
template <int CTRL> __device__ __forceinline__ float dppf(float x) {
  return __builtin_bit_cast(float, __builtin_amdgcn_mov_dpp(__builtin_bit_cast(int, x), CTRL, 0xf, 0xf, true));
}
constexpr int XOR1 = 0xB1, XOR2 = 0x4E, XOR7 = 0x141, XOR8 = 0x128, ROR4 = 0x124;
__device__ __forceinline__ float swap16_add(float a, float b) {
  auto r = __builtin_amdgcn_permlane16_swap(__float_as_uint(a), __float_as_uint(b), false, false);
  return __uint_as_float(r[0]) + __uint_as_float(r[1]);
}
__device__ __forceinline__ float swap32_add(float a, float b) {
  auto r = __builtin_amdgcn_permlane32_swap(__float_as_uint(a), __float_as_uint(b), false, false);
  return __uint_as_float(r[0]) + __uint_as_float(r[1]);
}
__device__ __forceinline__ float swap32_max(float a) {
  auto r = __builtin_amdgcn_permlane32_swap(__float_as_uint(a), __float_as_uint(a), false, false);
  return fmaxf(__uint_as_float(r[0]), __uint_as_float(r[1]));
}
__device__ __forceinline__ float half_max(float v) {
  v = fmaxf(v, dppf<XOR1>(v));
  v = fmaxf(v, dppf<XOR2>(v));
  v = fmaxf(v, dppf<XOR7>(v));
  v = fmaxf(v, dppf<XOR8>(v));
  auto r = __builtin_amdgcn_permlane16_swap(__float_as_uint(v), __float_as_uint(v), false, false);
  return fmaxf(__uint_as_float(r[0]), __uint_as_float(r[1]));
}
__device__ __forceinline__ float half_sum(float v) {
  v += dppf<XOR1>(v);
  v += dppf<XOR2>(v);
  v += dppf<XOR7>(v);
  v += dppf<XOR8>(v);
  return swap16_add(v, v);
}
__device__ __forceinline__ float sel(bool c, float a, float b) {
  asm volatile("" : "+v"(a), "+v"(b));
  return c ? a : b;
}
__device__ __forceinline__ bool tagged16(v4u g, unsigned tag) {
  return __builtin_amdgcn_perm(g[1], g[0], 0x07060302u) == tag && __builtin_amdgcn_perm(g[3], g[2], 0x07060302u) == tag;
}
template <int I, int N, class F> __device__ __forceinline__ void static_for(F&& f) {
  if constexpr (I < N) {
    f(std::integral_constant<int, I>{});
    static_for<I + 1, N>(f);
  }
}
#define LAMBDA(v) [&](auto v) __attribute__((always_inline))
typedef __amdgpu_buffer_rsrc_t rsrc_t;
__device__ __forceinline__ rsrc_t mk_rsrc(const void* base, unsigned bytes) {
  return __builtin_amdgcn_make_buffer_rsrc((void*)base, 0, bytes, 0x00020000);
}
template <int AUX> __device__ __forceinline__ v4u bld(rsrc_t rs, unsigned voff, unsigned soff) {
  return __builtin_amdgcn_raw_buffer_load_b128(rs, voff, soff, AUX);
}
#define SB() __builtin_amdgcn_sched_barrier(0)
#define LDS_BARRIER()                                  \
  do {                                                 \
    asm volatile("s_waitcnt lgkmcnt(0)" ::: "memory"); \
    __builtin_amdgcn_s_barrier();                      \
    asm volatile("" ::: "memory");                     \
    SB();                                              \
  } while (0)

constexpr int pace_thr(int n, int depth) {
  const int dith16 = 2 * (((n & 1) << 2) | (n & 2) | ((n & 4) >> 2)) + 1;
  const int lag = depth == 2   ? -50
                  : depth <= 4 ? PACE_GAIN * (16 * (depth - 2) - dith16) / 16
                               : 2 * PACE_GAIN + (PACE_GAIN + 300 * n / 128) * (16 - dith16) / 16;
  return PACE_T0 + n * PACE_DUR / 128 + lag;
}
template <int N> __device__ __forceinline__ void throttle(unsigned t_start) {
  const int el = (int)((unsigned)__builtin_amdgcn_s_memrealtime() - t_start);
  asm volatile(
      "s_waitcnt vmcnt(5)\n\ts_cmpk_ge_i32 %0, %1\n\ts_cbranch_scc1 1f\n\ts_waitcnt vmcnt(3)\n\t"
      "s_cmpk_ge_i32 %0, %2\n\ts_cbranch_scc1 1f\n\ts_waitcnt vmcnt(2)\n\t"
      "s_cmpk_ge_i32 %0, %3\n\ts_cbranch_scc1 1f\n\ts_waitcnt vmcnt(1)\n\t"
      "s_cmpk_ge_i32 %0, %4\n\ts_cbranch_scc1 1f\n\ts_waitcnt vmcnt(0)\n1:" ::"s"(el),
      "i"(pace_thr(N, 5)), "i"(pace_thr(N, 4)), "i"(pace_thr(N, 3)), "i"(pace_thr(N, 2))
      : "scc");
}
template <int N> __device__ __forceinline__ void throttle_hold(unsigned t_start, int& lag, unsigned hold) {
  constexpr int D = PACE_T0 + N * PACE_DUR / 128;
  asm volatile(
      "s_cmp_lg_u32 %2, 0\n\ts_cbranch_scc1 2f\n\ts_memrealtime s[66:67]\n\ts_waitcnt lgkmcnt(0)\n\t"
      "s_sub_u32 %0, s66, %1\n\ts_sub_u32 %0, %0, %3\n2:\n\t"
      "s_waitcnt vmcnt(5)\n\ts_cmpk_ge_i32 %0, %4\n\ts_cbranch_scc1 1f\n\ts_waitcnt vmcnt(3)\n\t"
      "s_cmpk_ge_i32 %0, %5\n\ts_cbranch_scc1 1f\n\ts_waitcnt vmcnt(2)\n\t"
      "s_cmpk_ge_i32 %0, %6\n\ts_cbranch_scc1 1f\n\ts_waitcnt vmcnt(1)\n\t"
      "s_cmpk_ge_i32 %0, %7\n\ts_cbranch_scc1 1f\n\ts_waitcnt vmcnt(0)\n1:"
      : "+s"(lag)
      : "s"(t_start), "s"(hold), "i"(D), "i"(pace_thr(N, 5) - D), "i"(pace_thr(N, 4) - D), "i"(pace_thr(N, 3) - D), "i"(pace_thr(N, 2) - D)
      : "scc", "s66", "s67");
}
constexpr int issue_after(int g) {
  const int n = g < 2 * (WIN - DIST) ? ((g & 1) ? WIN + g / 2 : -1) : g + DIST;
  return n < 2 * NLD ? n : -1;
}

#define ST(k)
#define ST_FLUSH()

constexpr int L_QP = 0;
constexpr int L_P = L_QP + 8 * 64;
constexpr int L_ACC = L_P + 8 * 128;
constexpr int L_O = L_ACC + 8 * 256;
constexpr int L_ML = L_O + 128;
constexpr int L_CNT = L_ML + 16;
constexpr int L_END = L_CNT + 4;

__global__ void __launch_bounds__(NTHR, 2) mha2_decode_kernel(Params prm) {
  __shared__ __attribute__((aligned(16))) float lds[L_END];
  ST(0);
  const unsigned t_start = (unsigned)__builtin_amdgcn_s_memrealtime();
  const int tid = threadIdx.x, lane = tid & 63;
  const int w = __builtin_amdgcn_readfirstlane(tid >> 6);
  const int bid = blockIdx.x;
  const int team = (bid & 7) * 4 + (bid >> 6);
  const int blk = team >> 4, h = team & 15;
  const int r = (bid >> 3) & 7, b = r >> 2, s = r & 3;
  const int hh = lane >> 5, l32 = lane & 31, rc = lane >> 3, c8 = lane & 7;
  const int grp = blk * 2 + b, me = s * 16 + h;
  const char* const pk = (const char*)(blk ? prm.k[1] : prm.k[0]);
  const char* const pv = (const char*)(blk ? prm.v[1] : prm.v[0]);
  const char* const pwq = (const char*)(blk ? prm.wq[1] : prm.wq[0]);
  const char* const pwo = (const char*)(blk ? prm.wo[1] : prm.wo[0]);
  const char* const px = (const char*)(blk ? prm.x[1] : prm.x[0]);

  const size_t kvoff = (size_t)(b * H + h) * SK * DH * 2;
  const rsrc_t rk = mk_rsrc(pk + kvoff, SK * DH * 2);
  const rsrc_t rv = mk_rsrc(pv + kvoff, SK * DH * 2);
  unsigned kvb = (unsigned)(grp * 16 + h) * 32768u + ((w & 1) << 9) + ((w & 6) << 11) + ((s & 1) << 14) + ((s & 2) << 15);
  unsigned kvo = 0;
  const unsigned l16 = lane * 16;
  const rsrc_t rw = mk_rsrc(prm.ws, WS_END);
  const rsrc_t rq = mk_rsrc(pwq + (size_t)(w * 56) * (H * DH * 2) + h * (DH * 2) + r * 64, 55 * (H * DH * 2) + 64);
  const rsrc_t rx = mk_rsrc(px + w * 112, DM * 2 + 112);
  const rsrc_t ro = mk_rsrc(pwo + (size_t)h * DH * (DM * 2) + w * 128, w < 7 ? 255 * (DM * 2) + 128 : 0);

  asm volatile("" ::"s"(pk));
  SB();
  int ampk = (int)((unsigned)__builtin_amdgcn_s_memrealtime() - t_start);
  SB();
  unsigned tag;
  v4u ring[WIN];
  v4u wqv[4], qg, ml;
  v2u x0, x1;
  int lag = 0;
  unsigned tv;
  const unsigned hold = (unsigned)__builtin_amdgcn_readfirstlane(((w + 1) >> 3) & ~(s & (s >> 1)) & 1);
  v4u wb[32];
  v8u bt[8][2];

  auto q_share = [&]() __attribute__((always_inline)) {
    float a0[8], a1[8];
#pragma unroll
    for (int j = 0; j < 4; ++j) {
      const unsigned p0 = __builtin_amdgcn_perm(wqv[1][j], wqv[0][j], 0x05040100u), p1 = __builtin_amdgcn_perm(wqv[1][j], wqv[0][j], 0x07060302u);
      const unsigned p2 = __builtin_amdgcn_perm(wqv[3][j], wqv[2][j], 0x05040100u), p3 = __builtin_amdgcn_perm(wqv[3][j], wqv[2][j], 0x07060302u);
      a0[2 * j] = dot2(p2, x0[1], dot2(p0, x0[0], 0.f));
      a0[2 * j + 1] = dot2(p3, x0[1], dot2(p1, x0[0], 0.f));
      a1[2 * j] = dot2(p2, x1[1], dot2(p0, x1[0], 0.f));
      a1[2 * j + 1] = dot2(p3, x1[1], dot2(p1, x1[0], 0.f));
    }
    float z[8];
#pragma unroll
    for (int j = 0; j < 8; ++j) z[j] = swap32_add(a0[j], a1[j]);
    f32x4 u;
#pragma unroll
    for (int j = 0; j < 4; ++j) {
      float t = swap16_add(z[j], z[j + 4]);
      t += dppf<ROR4>(t);
      t += dppf<XOR8>(t);
      u[j] = t;
    }
    if ((lane & 12) == 0) *(f32x4*)(lds + L_QP + w * 64 + hh * 32 + (lane & 3) * 8 + ((lane >> 4) & 1) * 4) = u;
    ST(1);
    unsigned arrived = 0;
    asm volatile("s_waitcnt lgkmcnt(0)" : "+s"(tag)::"memory");
    if (lane == 0) arrived = __hip_atomic_fetch_add((unsigned*)lds + L_CNT, 1u, __ATOMIC_RELAXED, __HIP_MEMORY_SCOPE_WORKGROUP);
    asm volatile("" ::: "memory");
    if (__builtin_amdgcn_readfirstlane(arrived) == 7) {
      float q = 0.f;
#pragma unroll
      for (int i = 0; i < 8; ++i) q += lds[L_QP + i * 64 + lane];
      __builtin_amdgcn_raw_buffer_store_b32((pack_bf16(q, q) & 0xffffu) | (((lane & 1) ? tag : tag << 16) & 0xffff0000u), rw,
                                            WS_Q + (team * 2 + hh) * 1024 + ((lane >> 2) & 1) * 512 + (r * 4 + (l32 >> 3)) * 16 + (lane & 3) * 4, 0, AUX_SC1);
    }
    SB();
  };

  auto issue = LAMBDA(nc) {
    constexpr int n = decltype(nc)::value;
    if constexpr (n == N_WQ) {
      if (tid < 2) ((unsigned*)lds)[L_CNT + tid] = 0u;
      LDS_BARRIER();
      tag = *(const CAS unsigned*)((const CAS char*)prm.ws + WS_EPOCH + blk * 256) + 0x01010101u;
      const unsigned xoff = lane < 56 ? (lane >> 2) * 8 : OOB;
      x0 = __builtin_amdgcn_raw_buffer_load_b64(rx, xoff, 0, 0);
      x1 = __builtin_amdgcn_raw_buffer_load_b64(rx, xoff, DM * 2, 0);
#pragma unroll
      for (int i = 0; i < 4; ++i) wqv[i] = bld<0>(rq, (lane >> 2) * 4 * (H * DH * 2) + (lane & 3) * 16, i * (H * DH * 2));
    }
    if constexpr (n == N_QCALC) q_share();
    if constexpr (n >= FREE_N) throttle<n>(t_start);
    if constexpr (n == N_QPOLL) qg = bld<AUX_SC1>(rw, WS_Q + (team * 2 + b) * 1024 + l16, 0);
    if constexpr (n == N_ML) ml = bld<AUX_SC1>(rw, WS_ML + (grp * 16 + h) * 512 + l32 * 16, 0);
    SB();
    if constexpr ((n & 1) == 0) {
      asm volatile("" : "+s"(kvb));
      kvo = (kvb + ((n & 2) << 14) + ((n & 60) << 15)) & (SK * DH * 2 - 1);
    }
    ring[n % WIN] = bld<AUX_NT>(n < NLD ? rk : rv, l32 * 16 + hh * 2048 + (n & 1) * 1024, kvo);
    SB();
    static_for<0, 32>(LAMBDA(jc) {
      constexpr int j = decltype(jc)::value;
      if constexpr (n == (j < 4 ? WM0 + j * WMS : WO0 + 3 * j)) {
        wb[j] = bld<0>(ro, 32 * rc * (DM * 2) + c8 * 16 + (j & 3) * (DM * 2), (unsigned)(((j >> 2) ^ r) * 4 * (DM * 2)));
        SB();
      }
    });
    constexpr int P0 = WO0 + DIST + 1;
    static_assert(P0 + 31 * 3 < 2 * NLD, "all wo rows are paired inside the stream");
    if constexpr (n >= P0 && (n - P0) % 3 == 0 && (n - P0) / 3 < 32 && (((n - P0) / 3) & 1)) {
      constexpr int j = (n - P0) / 3, t = j >> 1;
#pragma unroll
      for (int jj = 0; jj < 4; ++jj) {
        unsigned lo = __builtin_amdgcn_perm(wb[2 * t + 1][jj], wb[2 * t][jj], 0x05040100u);
        unsigned hi = __builtin_amdgcn_perm(wb[2 * t + 1][jj], wb[2 * t][jj], 0x07060302u);
        asm volatile("" : "+v"(lo), "+v"(hi));
        bt[2 * jj][t >> 3][t & 7] = lo;
        bt[2 * jj + 1][t >> 3][t & 7] = hi;
      }
      if constexpr (j == 31) {
#pragma unroll
        for (int jj = 0; jj < 8; ++jj) asm volatile("" : "+v"(bt[jj][0]), "+v"(bt[jj][1]));
      }
      SB();
    }
  };
  auto after = LAMBDA(gc) {
    constexpr int n = issue_after(decltype(gc)::value);
    SB();
    if constexpr (n >= 0) issue(std::integral_constant<int, n>{});
  };

  static_for<0, WIN>(issue);
  if (__builtin_expect(!__all(tagged16(qg, tag)), 0)) {
    for (unsigned spins = 0; spins < SPIN_CAP; ++spins) {
      asm volatile("" ::: "memory");
      qg = bld<AUX_SC1>(rw, WS_Q + (team * 2 + b) * 1024 + l16, 0);
      if (__all(tagged16(qg, tag))) break;
    }
  }
  v4u qv;
  {
    const unsigned q0 = __builtin_amdgcn_perm(qg[1], qg[0], 0x05040100u), q1 = __builtin_amdgcn_perm(qg[3], qg[2], 0x05040100u);
    auto e = __builtin_amdgcn_permlane32_swap(q0, q0, false, false);
    auto o = __builtin_amdgcn_permlane32_swap(q1, q1, false, false);
    qv = v4u{e[0], o[0], e[1], o[1]};
  }
  ST(2);

  float sc[2];
  {
    float xs[16];
    float red[2];
    static_for<0, NLD>(LAMBDA(tc) {
      constexpr int t = decltype(tc)::value;
      const v4u cur = ring[t % WIN];
      xs[t & 15] = dot2(cur[3], qv[3], dot2(cur[2], qv[2], dot2(cur[1], qv[1], dot2(cur[0], qv[0], 0.f))));
      asm volatile("" : "+v"(xs[t & 15]));
      after(tc);
      if constexpr ((t & 15) == 15) {
#define TR_STEP(HALF, CTRL)                                               \
  _Pragma("unroll") for (int u = 0; u < (HALF); ++u) {                    \
    const float lo_ = xs[u], hi_ = xs[u + (HALF)];                        \
    const float t1_ = lo_ + dppf<CTRL>(lo_), t2_ = hi_ + dppf<CTRL>(hi_); \
    xs[u] = (lane & (HALF)) ? t2_ : t1_;                                  \
  }
        TR_STEP(8, XOR8) TR_STEP(4, XOR7) TR_STEP(2, XOR2) TR_STEP(1, XOR1)
#undef TR_STEP
        asm volatile("" : "+v"(xs[0]));
        red[(t >> 4) & 1] = xs[0];
        if constexpr ((t & 31) == 31) sc[t >> 5] = swap16_add(red[0], red[1]);
        SB();
      }
    });
  }
  ST(3);
  constexpr float C2 = 0.0625f * 1.4426950408889634f;
  const float mw = swap32_max(half_max(fmaxf(sc[0], sc[1]))) * C2;
  {
    const float p0 = __builtin_amdgcn_exp2f(fmaf(sc[0], C2, -mw));
    const float p1 = __builtin_amdgcn_exp2f(fmaf(sc[1], C2, -mw));
    lds[L_P + w * 128 + hh * 64 + l32] = p0;
    lds[L_P + w * 128 + hh * 64 + 32 + l32] = p1;
    float lw = half_sum(p0 + p1);
    lw = swap32_add(lw, lw);
    unsigned arrived = 0;
    if (lane == 0) {
      *(f32x2*)(lds + L_ML + w * 2) = f32x2{mw, lw};
      asm volatile("" ::: "memory");
      arrived = __hip_atomic_fetch_add((unsigned*)lds + L_CNT + 1, 1u, __ATOMIC_RELAXED, __HIP_MEMORY_SCOPE_WORKGROUP);
    }
    asm volatile("" ::: "memory");
    if (__builtin_amdgcn_readfirstlane(arrived) == 7) {
      const v2u e = *(const v2u*)(lds + L_ML + c8 * 2);
      __builtin_amdgcn_raw_buffer_store_b128(v4u{__builtin_amdgcn_perm(tag, e[0], 0x05040100u), __builtin_amdgcn_perm(tag, e[0], 0x07060302u),
                                                 __builtin_amdgcn_perm(tag, e[1], 0x05040100u), __builtin_amdgcn_perm(tag, e[1], 0x07060302u)}, rw,
                                             (lane < 8 ? WS_ML : OOB) + (grp * 16 + h) * 512 + (s * 8 + c8) * 16, 0, AUX_SC1);
    }
  }
  unsigned yst = (w < 7 ? WS_Y : OOB) + ((grp * 14 + w * 2 + hh) * 64 + me) * 128 + l32 * 4;
  unsigned ytag = (tag >> ((lane & 3) * 8)) & 255u;
  unsigned accrd = L_ACC * 4 + rc * 1024 + w * 128 + c8 * 16;
  const int kk = c8 ^ r;
  unsigned owr = L_O * 4 + (((kk >> 2) * 4 + ((kk >> 1) & 1) * 2 + (w >> 2)) * 16 + hh * 8 + (w & 1) * 4 + ((w >> 1) & 1) * 2 + (kk & 1)) * 4;
  asm volatile("s_waitcnt lgkmcnt(0)" : "+v"(yst), "+v"(ytag), "+v"(accrd), "+v"(owr)::"memory");
  SB();

  f32x2 acc[4];
#pragma unroll
  for (int j = 0; j < 4; ++j) acc[j] = f32x2{0.f, 0.f};
  float fw;
  bool mlok;
  auto ml_eval = [&]() __attribute__((always_inline)) {
    mlok = __all(tagged16(ml, tag));
    const float m = __uint_as_float(__builtin_amdgcn_perm(ml[1], ml[0], 0x05040100u));
    const float M = half_max(m);
    const float L = half_sum(__builtin_amdgcn_exp2f(m - M) * __uint_as_float(__builtin_amdgcn_perm(ml[3], ml[2], 0x05040100u)));
    fw = __builtin_amdgcn_exp2f(mw - M) / L;
  };
  {
    const f32x4* pl = (const f32x4*)(lds + L_P + w * 128 + hh * 64);
    f32x4 pc = pl[0];
    static_for<0, NLD>(LAMBDA(tc) {
      constexpr int t = decltype(tc)::value;
      const v4u cur = ring[(t + NLD) % WIN];
      const float pv_ = pc[t & 3];
      if constexpr ((t & 3) == 3 && t + 1 < NLD) pc = pl[(t + 1) >> 2];
#pragma unroll
      for (int j = 0; j < 4; ++j) {
        const f32x2 vv = {__uint_as_float(cur[j] << 16), __uint_as_float(cur[j] & 0xffff0000u)};
        acc[j] = __builtin_elementwise_fma(vv, f32x2{pv_, pv_}, acc[j]);
      }
      asm volatile("" : "+v"(acc[0]), "+v"(acc[1]), "+v"(acc[2]), "+v"(acc[3]));
      after(std::integral_constant<int, t + NLD>{});
      if constexpr (t + NLD == G_ML) {
        ml_eval();
        asm volatile("" : "+v"(fw));
        SB();
      }
    });
  }
  if (__builtin_expect(!mlok, 0)) {
    for (unsigned spins = 0; !mlok && spins < SPIN_CAP; ++spins) {
      __builtin_amdgcn_s_sleep(8);
      asm volatile("" ::: "memory");
      ml = bld<AUX_SC1>(rw, WS_ML + (grp * 16 + h) * 512 + l32 * 16, 0);
      ml_eval();
    }
  }
  ST(4);

  {
    f32x4 rr;
#pragma unroll
    for (int j = 0; j < 4; ++j) rr[j] = swap32_add(acc[j >> 1][j & 1], acc[2 + (j >> 1)][j & 1]) * fw;
    *(f32x4*)(lds + L_ACC + w * 256 + l32 * 8 + hh * 4) = rr;
  }
  LDS_BARRIER();
  const f32x4 a = *(const f32x4*)((const char*)lds + accrd);
  SB();
  int sidx = (lane & 2) ? (int)0xEEEEEEEEu : 0x44444444;
  unsigned ord = L_O * 4 + (lane >> 4) * 64 + (lane & 3) * 16;
  f32x4 dd[8];
#pragma unroll
  for (int j = 0; j < 8; ++j) {
    dd[j] = f32x4{0.f, 0.f, 0.f, 0.f};
    asm volatile("" : "+v"(dd[j]));
  }
  asm volatile("" : "+v"(sidx), "+v"(ord));
  SB();
  {
    float e0 = swap32_add(a[0], a[2]), e1 = swap32_add(a[1], a[3]);
    e0 += dppf<XOR8>(e0);
    e1 += dppf<XOR8>(e1);
    e0 = swap16_add(e0, e0);
    e1 = swap16_add(e1, e1);
    if ((lane & 24) == 0) *(unsigned*)((char*)lds + owr) = pack_bf16(e0, e1);
  }
  LDS_BARRIER();

  {
    const v4u oa0 = *(const v4u*)((const char*)lds + ord), oa1 = *(const v4u*)((const char*)lds + ord + 256);
#pragma unroll
    for (int j = 0; j < 8; ++j)
      dd[j] = __builtin_amdgcn_smfmac_f32_16x16x64_bf16(__builtin_bit_cast(bf16x8, oa0), __builtin_bit_cast(bf16x16, bt[j][0]), dd[j], sidx, 0, 0);
#pragma unroll
    for (int j = 0; j < 8; ++j)
      dd[j] = __builtin_amdgcn_smfmac_f32_16x16x64_bf16(__builtin_bit_cast(bf16x8, oa1), __builtin_bit_cast(bf16x16, bt[j][1]), dd[j], sidx, 0, 0);
    SB();
    float y[8];
#pragma unroll
    for (int j = 0; j < 8; ++j) y[j] = sel(lane & 8, dd[j][1] + dd[j][3], dd[j][0] + dd[j][2]);
    float u[2];
#pragma unroll
    for (int j = 0; j < 2; ++j) u[j] = sel(hh, sel(lane & 16, y[6 + j], y[4 + j]), sel(lane & 16, y[2 + j], y[j]));
    const float t0 = u[0] + dppf<XOR8>(u[0]), t1 = u[1] + dppf<XOR8>(u[1]);
    const float yv = (lane & 8) ? t1 : t0;
    __builtin_amdgcn_raw_buffer_store_b32((__float_as_uint(yv) & ~255u) | ytag, rw, yst, 0, AUX_SC1);
    ST(5);
  }
  if (w < 7 || me >= 14) { ST_FLUSH(); return; }

  {
    const unsigned yo = WS_Y + (grp * 14 + me) * 8192 + l16;
    const v4u tg = {tag & 255u, (tag >> 8) & 255u, (tag >> 16) & 255u, tag >> 24};
    const int ls = 32 * (me & 1) + 4 * c8 + 2 * hh + ((lane >> 4) & 1);
    GAS unsigned short* optr = (GAS unsigned short*)prm.out + b * (2 * DM) + blk * DM + 64 * (me >> 1) + 8 * (ls & 7) + (ls >> 3);
    __builtin_amdgcn_s_sleep(16);
    f32x4 sum;
    for (unsigned spins = 0;; ++spins) {
      v4u g[8];
#pragma unroll
      for (int t = 0; t < 8; ++t) g[t] = bld<AUX_SC1>(rw, yo, t * 1024);
      unsigned bad = 0;
      sum = f32x4{0.f, 0.f, 0.f, 0.f};
#pragma unroll
      for (int t = 0; t < 8; ++t) {
        const v4u x = g[t] ^ tg;
        bad |= (x[0] | x[1]) | (x[2] | x[3]);
        sum += __builtin_bit_cast(f32x4, x);
      }
      if (__all((bad & 255u) == 0) || spins > SPIN_CAP) break;
      asm volatile("" ::: "memory");
    }
    ST(6);
    float rr = swap16_add(swap32_add(sum[0], sum[2]), swap32_add(sum[1], sum[3]));
    rr += dppf<XOR8>(rr);
    if ((lane & 8) == 0) __hip_atomic_store(optr, (unsigned short)(pack_bf16(rr, rr) & 0xffffu), RLX_AGENT);
    if (me == 0 && b == 0 && lane == 0) __hip_atomic_store((GAS unsigned*)((GAS char*)prm.ws + WS_EPOCH + blk * 256), tag, RLX_AGENT);
    {
      const int el = (int)((unsigned)__builtin_amdgcn_s_memrealtime() - t_start);
      const int tgt = 10000 + 10 * ampk;
      while ((int)((unsigned)__builtin_amdgcn_s_memrealtime() - t_start) < tgt) __builtin_amdgcn_s_sleep(1);
    }
    ST(7);
    ST_FLUSH();
  }
}

extern "C" void kernel_launch(void* const* d_in, const int* in_sizes, int n_in, void* d_out, int out_size, void* d_ws,
                              size_t ws_size, hipStream_t stream) {
  Params p;
  p.x[0] = d_in[0];
  p.k[0] = d_in[1];
  p.v[0] = d_in[2];
  p.wq[0] = d_in[3];
  p.wo[0] = d_in[4];
  p.x[1] = d_in[5];
  p.k[1] = d_in[6];
  p.v[1] = d_in[7];
  p.wq[1] = d_in[8];
  p.wo[1] = d_in[9];
  p.out = d_out;
  p.ws = d_ws;
  mha2_decode_kernel<<<dim3(256), dim3(NTHR), 0, stream>>>(p);
}
```

```cpp
#define AMP
#define P_T0 -10
#include <hip/hip_runtime.h>
#include <hip/hip_bf16.h>
#include <stdint.h>
#include <type_traits>

#define GAS __attribute__((address_space(1)))
#define CAS __attribute__((address_space(4)))
typedef unsigned v2u __attribute__((ext_vector_type(2)));
typedef unsigned v4u __attribute__((ext_vector_type(4)));
typedef unsigned v8u __attribute__((ext_vector_type(8)));
typedef float f32x2 __attribute__((ext_vector_type(2)));
typedef float f32x4 __attribute__((ext_vector_type(4)));
typedef __bf16 bf16x2 __attribute__((ext_vector_type(2)));
typedef __bf16 bf16x8 __attribute__((ext_vector_type(8)));
typedef __bf16 bf16x16 __attribute__((ext_vector_type(16)));
#define RLX_AGENT __ATOMIC_RELAXED, __HIP_MEMORY_SCOPE_AGENT

constexpr int WIN = 16;
constexpr int DIST = 6;
constexpr int FREE_N = 4;
constexpr int N_WQ = 2;
constexpr int N_QCALC = 6;
constexpr int N_QPOLL = 13;
constexpr int WM0 = 6, WMS = 2;
constexpr int WO0 = 12;
constexpr int N_ML = 96;
constexpr int G_ML = 123;
constexpr int PACE_T0 = P_T0;
constexpr int PACE_DUR = 3560;
constexpr int PACE_GAIN = 128;

constexpr int H = 16, SK = 4096, DH = 256, DM = 448;
constexpr int NTHR = 512;
constexpr int NLD = 64;
constexpr unsigned WS_EPOCH = 0;
constexpr unsigned WS_Q = 4096;
constexpr unsigned WS_ML = WS_Q + 65536;
constexpr unsigned WS_Y = WS_ML + 32768;
constexpr unsigned WS_END = WS_Y + 4 * 14 * 8192;
constexpr unsigned SPIN_CAP = 1u << 20;
constexpr unsigned OOB = 0x7fffff00u;
constexpr int AUX_NT = 2, AUX_SC1 = 16;

struct Params {
  const void* k[2];
  const void* wq[2];
  const void* x[2];
  void* ws;
  void* out;
  const void* v[2];
  const void* wo[2];
};

__device__ __forceinline__ float dot2(unsigned a, unsigned b, float c) {
  return __builtin_amdgcn_fdot2_f32_bf16(__builtin_bit_cast(bf16x2, a), __builtin_bit_cast(bf16x2, b), c, false);
}
__device__ __forceinline__ unsigned pack_bf16(float lo, float hi) {
  bf16x2 r;
  r.x = (__bf16)lo;
  r.y = (__bf16)hi;
  return __builtin_bit_cast(unsigned, r);
}
template <int CTRL> __device__ __forceinline__ float dppf(float x) {
  return __builtin_bit_cast(float, __builtin_amdgcn_mov_dpp(__builtin_bit_cast(int, x), CTRL, 0xf, 0xf, true));
}
constexpr int XOR1 = 0xB1, XOR2 = 0x4E, XOR7 = 0x141, XOR8 = 0x128, ROR4 = 0x124;
__device__ __forceinline__ float swap16_add(float a, float b) {
  auto r = __builtin_amdgcn_permlane16_swap(__float_as_uint(a), __float_as_uint(b), false, false);
  return __uint_as_float(r[0]) + __uint_as_float(r[1]);
}
__device__ __forceinline__ float swap32_add(float a, float b) {
  auto r = __builtin_amdgcn_permlane32_swap(__float_as_uint(a), __float_as_uint(b), false, false);
  return __uint_as_float(r[0]) + __uint_as_float(r[1]);
}
__device__ __forceinline__ float swap32_max(float a) {
  auto r = __builtin_amdgcn_permlane32_swap(__float_as_uint(a), __float_as_uint(a), false, false);
  return fmaxf(__uint_as_float(r[0]), __uint_as_float(r[1]));
}
__device__ __forceinline__ float half_max(float v) {
  v = fmaxf(v, dppf<XOR1>(v));
  v = fmaxf(v, dppf<XOR2>(v));
  v = fmaxf(v, dppf<XOR7>(v));
  v = fmaxf(v, dppf<XOR8>(v));
  auto r = __builtin_amdgcn_permlane16_swap(__float_as_uint(v), __float_as_uint(v), false, false);
  return fmaxf(__uint_as_float(r[0]), __uint_as_float(r[1]));
}
__device__ __forceinline__ float half_sum(float v) {
  v += dppf<XOR1>(v);
  v += dppf<XOR2>(v);
  v += dppf<XOR7>(v);
  v += dppf<XOR8>(v);
  return swap16_add(v, v);
}
__device__ __forceinline__ float sel(bool c, float a, float b) {
  asm volatile("" : "+v"(a), "+v"(b));
  return c ? a : b;
}
__device__ __forceinline__ bool tagged16(v4u g, unsigned tag) {
  return __builtin_amdgcn_perm(g[1], g[0], 0x07060302u) == tag && __builtin_amdgcn_perm(g[3], g[2], 0x07060302u) == tag;
}
template <int I, int N, class F> __device__ __forceinline__ void static_for(F&& f) {
  if constexpr (I < N) {
    f(std::integral_constant<int, I>{});
    static_for<I + 1, N>(f);
  }
}
#define LAMBDA(v) [&](auto v) __attribute__((always_inline))
typedef __amdgpu_buffer_rsrc_t rsrc_t;
__device__ __forceinline__ rsrc_t mk_rsrc(const void* base, unsigned bytes) {
  return __builtin_amdgcn_make_buffer_rsrc((void*)base, 0, bytes, 0x00020000);
}
template <int AUX> __device__ __forceinline__ v4u bld(rsrc_t rs, unsigned voff, unsigned soff) {
  return __builtin_amdgcn_raw_buffer_load_b128(rs, voff, soff, AUX);
}
#define SB() __builtin_amdgcn_sched_barrier(0)
#define LDS_BARRIER()                                  \
  do {                                                 \
    asm volatile("s_waitcnt lgkmcnt(0)" ::: "memory"); \
    __builtin_amdgcn_s_barrier();                      \
    asm volatile("" ::: "memory");                     \
    SB();                                              \
  } while (0)

constexpr int pace_thr(int n, int depth) {
  const int dith16 = 2 * (((n & 1) << 2) | (n & 2) | ((n & 4) >> 2)) + 1;
  const int lag = depth == 2   ? -50
                  : depth <= 4 ? PACE_GAIN * (16 * (depth - 2) - dith16) / 16
                               : 2 * PACE_GAIN + (PACE_GAIN + 300 * n / 128) * (16 - dith16) / 16;
  return PACE_T0 + n * PACE_DUR / 128 + lag;
}
template <int N> __device__ __forceinline__ void throttle(unsigned t_start) {
  const int el = (int)((unsigned)__builtin_amdgcn_s_memrealtime() - t_start);
  asm volatile(
      "s_waitcnt vmcnt(5)\n\ts_cmpk_ge_i32 %0, %1\n\ts_cbranch_scc1 1f\n\ts_waitcnt vmcnt(3)\n\t"
      "s_cmpk_ge_i32 %0, %2\n\ts_cbranch_scc1 1f\n\ts_waitcnt vmcnt(2)\n\t"
      "s_cmpk_ge_i32 %0, %3\n\ts_cbranch_scc1 1f\n\ts_waitcnt vmcnt(1)\n\t"
      "s_cmpk_ge_i32 %0, %4\n\ts_cbranch_scc1 1f\n\ts_waitcnt vmcnt(0)\n1:" ::"s"(el),
      "i"(pace_thr(N, 5)), "i"(pace_thr(N, 4)), "i"(pace_thr(N, 3)), "i"(pace_thr(N, 2))
      : "scc");
}
constexpr int issue_after(int g) {
  const int n = g < 2 * (WIN - DIST) ? ((g & 1) ? WIN + g / 2 : -1) : g + DIST;
  return n < 2 * NLD ? n : -1;
}

#define ST(k)
#define ST_FLUSH()

constexpr int L_QP = 0;
constexpr int L_P = L_QP + 8 * 64;
constexpr int L_ACC = L_P + 8 * 128;
constexpr int L_O = L_ACC + 8 * 256;
constexpr int L_ML = L_O + 128;
constexpr int L_CNT = L_ML + 16;
constexpr int L_END = L_CNT + 4;

__global__ void __launch_bounds__(NTHR, 2) mha2_decode_kernel(Params prm) {
  __shared__ __attribute__((aligned(16))) float lds[L_END];
  ST(0);
  const unsigned t_amp = (unsigned)__builtin_amdgcn_s_memrealtime();
  unsigned t_start;
  const int tid = threadIdx.x, lane = tid & 63;
  const int w = __builtin_amdgcn_readfirstlane(tid >> 6);
  const int bid = blockIdx.x;
  const int team = (bid & 7) * 4 + (bid >> 6);
  const int blk = team >> 4, h = team & 15;
  const int r = (bid >> 3) & 7, b = r >> 2, s = r & 3;
  const int hh = lane >> 5, l32 = lane & 31, rc = lane >> 3, c8 = lane & 7;
  const int grp = blk * 2 + b, me = s * 16 + h;
  const char* const pk = (const char*)(blk ? prm.k[1] : prm.k[0]);
  const char* const pv = (const char*)(blk ? prm.v[1] : prm.v[0]);
  const char* const pwq = (const char*)(blk ? prm.wq[1] : prm.wq[0]);
  const char* const pwo = (const char*)(blk ? prm.wo[1] : prm.wo[0]);
  const char* const px = (const char*)(blk ? prm.x[1] : prm.x[0]);

  const size_t kvoff = (size_t)(b * H + h) * SK * DH * 2;
  const rsrc_t rk = mk_rsrc(pk + kvoff, SK * DH * 2);
  const rsrc_t rv = mk_rsrc(pv + kvoff, SK * DH * 2);
  unsigned kvb = (unsigned)(grp * 16 + h) * 32768u + ((w & 1) << 9) + ((w & 6) << 11) + ((s & 1) << 14) + ((s & 2) << 15);
  unsigned kvo = 0;
  const unsigned l16 = lane * 16;
  const rsrc_t rw = mk_rsrc(prm.ws, WS_END);
  const rsrc_t rq = mk_rsrc(pwq + (size_t)(w * 56) * (H * DH * 2) + h * (DH * 2) + r * 64, 55 * (H * DH * 2) + 64);
  const rsrc_t rx = mk_rsrc(px + w * 112, DM * 2 + 112);
  const rsrc_t ro = mk_rsrc(pwo + (size_t)h * DH * (DM * 2) + w * 128, w < 7 ? 255 * (DM * 2) + 128 : 0);

  unsigned tag;
  v4u ring[WIN];
  v4u wqv[4], qg, ml;
  v2u x0, x1;
  v4u wb[32];
  v8u bt[8][2];

  auto q_share = [&]() __attribute__((always_inline)) {
    float a0[8], a1[8];
#pragma unroll
    for (int j = 0; j < 4; ++j) {
      const unsigned p0 = __builtin_amdgcn_perm(wqv[1][j], wqv[0][j], 0x05040100u), p1 = __builtin_amdgcn_perm(wqv[1][j], wqv[0][j], 0x07060302u);
      const unsigned p2 = __builtin_amdgcn_perm(wqv[3][j], wqv[2][j], 0x05040100u), p3 = __builtin_amdgcn_perm(wqv[3][j], wqv[2][j], 0x07060302u);
      a0[2 * j] = dot2(p2, x0[1], dot2(p0, x0[0], 0.f));
      a0[2 * j + 1] = dot2(p3, x0[1], dot2(p1, x0[0], 0.f));
      a1[2 * j] = dot2(p2, x1[1], dot2(p0, x1[0], 0.f));
      a1[2 * j + 1] = dot2(p3, x1[1], dot2(p1, x1[0], 0.f));
    }
    float z[8];
#pragma unroll
    for (int j = 0; j < 8; ++j) z[j] = swap32_add(a0[j], a1[j]);
    f32x4 u;
#pragma unroll
    for (int j = 0; j < 4; ++j) {
      float t = swap16_add(z[j], z[j + 4]);
      t += dppf<ROR4>(t);
      t += dppf<XOR8>(t);
      u[j] = t;
    }
    if ((lane & 12) == 0) *(f32x4*)(lds + L_QP + w * 64 + hh * 32 + (lane & 3) * 8 + ((lane >> 4) & 1) * 4) = u;
    ST(1);
    unsigned arrived = 0;
    asm volatile("s_waitcnt lgkmcnt(0)" : "+s"(tag)::"memory");
    if (lane == 0) arrived = __hip_atomic_fetch_add((unsigned*)lds + L_CNT, 1u, __ATOMIC_RELAXED, __HIP_MEMORY_SCOPE_WORKGROUP);
    asm volatile("" ::: "memory");
    if (__builtin_amdgcn_readfirstlane(arrived) == 7) {
      float q = 0.f;
#pragma unroll
      for (int i = 0; i < 8; ++i) q += lds[L_QP + i * 64 + lane];
      __builtin_amdgcn_raw_buffer_store_b32((pack_bf16(q, q) & 0xffffu) | (((lane & 1) ? tag : tag << 16) & 0xffff0000u), rw,
                                            WS_Q + (team * 2 + hh) * 1024 + ((lane >> 2) & 1) * 512 + (r * 4 + (l32 >> 3)) * 16 + (lane & 3) * 4, 0, AUX_SC1);
    }
    SB();
  };

  auto issue = LAMBDA(nc) {
    constexpr int n = decltype(nc)::value;
    if constexpr (n == N_WQ) {
      t_start = (unsigned)__builtin_amdgcn_s_memrealtime();
      if (tid < 2) ((unsigned*)lds)[L_CNT + tid] = 0u;
      LDS_BARRIER();
      tag = *(const CAS unsigned*)((const CAS char*)prm.ws + WS_EPOCH + blk * 256) + 0x01010101u;
      const unsigned xoff = lane < 56 ? (lane >> 2) * 8 : OOB;
      x0 = __builtin_amdgcn_raw_buffer_load_b64(rx, xoff, 0, 0);
      x1 = __builtin_amdgcn_raw_buffer_load_b64(rx, xoff, DM * 2, 0);
#pragma unroll
      for (int i = 0; i < 4; ++i) wqv[i] = bld<0>(rq, (lane >> 2) * 4 * (H * DH * 2) + (lane & 3) * 16, i * (H * DH * 2));
    }
    if constexpr (n == N_QCALC) q_share();
    if constexpr (n >= FREE_N) throttle<n>(t_start);
    if constexpr (n == N_QPOLL) qg = bld<AUX_SC1>(rw, WS_Q + (team * 2 + b) * 1024 + l16, 0);
    if constexpr (n == N_ML) ml = bld<AUX_SC1>(rw, WS_ML + (grp * 16 + h) * 512 + l32 * 16, 0);
    SB();
    if constexpr ((n & 1) == 0) {
      asm volatile("" : "+s"(kvb));
      kvo = (kvb + ((n & 2) << 14) + ((n & 60) << 15)) & (SK * DH * 2 - 1);
    }
    ring[n % WIN] = bld<AUX_NT>(n < NLD ? rk : rv, l32 * 16 + hh * 2048 + (n & 1) * 1024, kvo);
    SB();
    static_for<0, 32>(LAMBDA(jc) {
      constexpr int j = decltype(jc)::value;
      if constexpr (n == (j < 4 ? WM0 + j * WMS : WO0 + 3 * j)) {
        wb[j] = bld<0>(ro, 32 * rc * (DM * 2) + c8 * 16 + (j & 3) * (DM * 2), (unsigned)(((j >> 2) ^ r) * 4 * (DM * 2)));
        SB();
      }
    });
    constexpr int P0 = WO0 + DIST + 1;
    static_assert(P0 + 31 * 3 < 2 * NLD, "all wo rows are paired inside the stream");
    if constexpr (n >= P0 && (n - P0) % 3 == 0 && (n - P0) / 3 < 32 && (((n - P0) / 3) & 1)) {
      constexpr int j = (n - P0) / 3, t = j >> 1;
#pragma unroll
      for (int jj = 0; jj < 4; ++jj) {
        unsigned lo = __builtin_amdgcn_perm(wb[2 * t + 1][jj], wb[2 * t][jj], 0x05040100u);
        unsigned hi = __builtin_amdgcn_perm(wb[2 * t + 1][jj], wb[2 * t][jj], 0x07060302u);
        asm volatile("" : "+v"(lo), "+v"(hi));
        bt[2 * jj][t >> 3][t & 7] = lo;
        bt[2 * jj + 1][t >> 3][t & 7] = hi;
      }
      if constexpr (j == 31) {
#pragma unroll
        for (int jj = 0; jj < 8; ++jj) asm volatile("" : "+v"(bt[jj][0]), "+v"(bt[jj][1]));
      }
      SB();
    }
  };
  auto after = LAMBDA(gc) {
    constexpr int n = issue_after(decltype(gc)::value);
    SB();
    if constexpr (n >= 0) issue(std::integral_constant<int, n>{});
  };

  static_for<0, WIN>(issue);
  if (__builtin_expect(!__all(tagged16(qg, tag)), 0)) {
    for (unsigned spins = 0; spins < SPIN_CAP; ++spins) {
      asm volatile("" ::: "memory");
      qg = bld<AUX_SC1>(rw, WS_Q + (team * 2 + b) * 1024 + l16, 0);
      if (__all(tagged16(qg, tag))) break;
    }
  }
  v4u qv;
  {
    const unsigned q0 = __builtin_amdgcn_perm(qg[1], qg[0], 0x05040100u), q1 = __builtin_amdgcn_perm(qg[3], qg[2], 0x05040100u);
    auto e = __builtin_amdgcn_permlane32_swap(q0, q0, false, false);
    auto o = __builtin_amdgcn_permlane32_swap(q1, q1, false, false);
    qv = v4u{e[0], o[0], e[1], o[1]};
  }
  ST(2);

  float sc[2];
  {
    float xs[16];
    float red[2];
    static_for<0, NLD>(LAMBDA(tc) {
      constexpr int t = decltype(tc)::value;
      const v4u cur = ring[t % WIN];
      xs[t & 15] = dot2(cur[3], qv[3], dot2(cur[2], qv[2], dot2(cur[1], qv[1], dot2(cur[0], qv[0], 0.f))));
      after(tc);
      if constexpr ((t & 15) == 15) {
#define TR_STEP(HALF, CTRL)                                               \
  _Pragma("unroll") for (int u = 0; u < (HALF); ++u) {                    \
    const float lo_ = xs[u], hi_ = xs[u + (HALF)];                        \
    const float t1_ = lo_ + dppf<CTRL>(lo_), t2_ = hi_ + dppf<CTRL>(hi_); \
    xs[u] = (lane & (HALF)) ? t2_ : t1_;                                  \
  }
        TR_STEP(8, XOR8) TR_STEP(4, XOR7) TR_STEP(2, XOR2) TR_STEP(1, XOR1)
#undef TR_STEP
        asm volatile("" : "+v"(xs[0]));
        red[(t >> 4) & 1] = xs[0];
        if constexpr ((t & 31) == 31) sc[t >> 5] = swap16_add(red[0], red[1]);
        SB();
      }
    });
  }
  ST(3);
  constexpr float C2 = 0.0625f * 1.4426950408889634f;
  const float mw = swap32_max(half_max(fmaxf(sc[0], sc[1]))) * C2;
  {
    const float p0 = __builtin_amdgcn_exp2f(fmaf(sc[0], C2, -mw));
    const float p1 = __builtin_amdgcn_exp2f(fmaf(sc[1], C2, -mw));
    lds[L_P + w * 128 + hh * 64 + l32] = p0;
    lds[L_P + w * 128 + hh * 64 + 32 + l32] = p1;
    float lw = half_sum(p0 + p1);
    lw = swap32_add(lw, lw);
    unsigned arrived = 0;
    if (lane == 0) {
      *(f32x2*)(lds + L_ML + w * 2) = f32x2{mw, lw};
      asm volatile("" ::: "memory");
      arrived = __hip_atomic_fetch_add((unsigned*)lds + L_CNT + 1, 1u, __ATOMIC_RELAXED, __HIP_MEMORY_SCOPE_WORKGROUP);
    }
    asm volatile("" ::: "memory");
    if (__builtin_amdgcn_readfirstlane(arrived) == 7) {
      const v2u e = *(const v2u*)(lds + L_ML + c8 * 2);
      __builtin_amdgcn_raw_buffer_store_b128(v4u{__builtin_amdgcn_perm(tag, e[0], 0x05040100u), __builtin_amdgcn_perm(tag, e[0], 0x07060302u),
                                                 __builtin_amdgcn_perm(tag, e[1], 0x05040100u), __builtin_amdgcn_perm(tag, e[1], 0x07060302u)}, rw,
                                             (lane < 8 ? WS_ML : OOB) + (grp * 16 + h) * 512 + (s * 8 + c8) * 16, 0, AUX_SC1);
    }
  }
  unsigned yst = (w < 7 ? WS_Y : OOB) + ((grp * 14 + w * 2 + hh) * 64 + me) * 128 + l32 * 4;
  unsigned ytag = (tag >> ((lane & 3) * 8)) & 255u;
  unsigned accrd = L_ACC * 4 + rc * 1024 + w * 128 + c8 * 16;
  const int kk = c8 ^ r;
  unsigned owr = L_O * 4 + (((kk >> 2) * 4 + ((kk >> 1) & 1) * 2 + (w >> 2)) * 16 + hh * 8 + (w & 1) * 4 + ((w >> 1) & 1) * 2 + (kk & 1)) * 4;
  asm volatile("s_waitcnt lgkmcnt(0)" : "+v"(yst), "+v"(ytag), "+v"(accrd), "+v"(owr)::"memory");
  SB();

  f32x2 acc[4];
#pragma unroll
  for (int j = 0; j < 4; ++j) acc[j] = f32x2{0.f, 0.f};
  float fw;
  bool mlok;
  auto ml_eval = [&]() __attribute__((always_inline)) {
    mlok = __all(tagged16(ml, tag));
    const float m = __uint_as_float(__builtin_amdgcn_perm(ml[1], ml[0], 0x05040100u));
    const float M = half_max(m);
    const float L = half_sum(__builtin_amdgcn_exp2f(m - M) * __uint_as_float(__builtin_amdgcn_perm(ml[3], ml[2], 0x05040100u)));
    fw = __builtin_amdgcn_exp2f(mw - M) / L;
  };
  {
    const f32x4* pl = (const f32x4*)(lds + L_P + w * 128 + hh * 64);
    f32x4 pc = pl[0];
    static_for<0, NLD>(LAMBDA(tc) {
      constexpr int t = decltype(tc)::value;
      const v4u cur = ring[(t + NLD) % WIN];
      const float pv_ = pc[t & 3];
      if constexpr ((t & 3) == 3 && t + 1 < NLD) pc = pl[(t + 1) >> 2];
#pragma unroll
      for (int j = 0; j < 4; ++j) {
        const f32x2 vv = {__uint_as_float(cur[j] << 16), __uint_as_float(cur[j] & 0xffff0000u)};
        acc[j] = __builtin_elementwise_fma(vv, f32x2{pv_, pv_}, acc[j]);
      }
      asm volatile("" : "+v"(acc[0]), "+v"(acc[1]), "+v"(acc[2]), "+v"(acc[3]));
      after(std::integral_constant<int, t + NLD>{});
      if constexpr (t + NLD == G_ML) {
        ml_eval();
        asm volatile("" : "+v"(fw));
        SB();
      }
    });
  }
  if (__builtin_expect(!mlok, 0)) {
    for (unsigned spins = 0; !mlok && spins < SPIN_CAP; ++spins) {
      __builtin_amdgcn_s_sleep(8);
      asm volatile("" ::: "memory");
      ml = bld<AUX_SC1>(rw, WS_ML + (grp * 16 + h) * 512 + l32 * 16, 0);
      ml_eval();
    }
  }
  ST(4);

  {
    f32x4 rr;
#pragma unroll
    for (int j = 0; j < 4; ++j) rr[j] = swap32_add(acc[j >> 1][j & 1], acc[2 + (j >> 1)][j & 1]) * fw;
    *(f32x4*)(lds + L_ACC + w * 256 + l32 * 8 + hh * 4) = rr;
  }
  LDS_BARRIER();
  const f32x4 a = *(const f32x4*)((const char*)lds + accrd);
  SB();
  int sidx = (lane & 2) ? (int)0xEEEEEEEEu : 0x44444444;
  unsigned ord = L_O * 4 + (lane >> 4) * 64 + (lane & 3) * 16;
  f32x4 dd[8];
#pragma unroll
  for (int j = 0; j < 8; ++j) {
    dd[j] = f32x4{0.f, 0.f, 0.f, 0.f};
    asm volatile("" : "+v"(dd[j]));
  }
  asm volatile("" : "+v"(sidx), "+v"(ord));
  SB();
  {
    float e0 = swap32_add(a[0], a[2]), e1 = swap32_add(a[1], a[3]);
    e0 += dppf<XOR8>(e0);
    e1 += dppf<XOR8>(e1);
    e0 = swap16_add(e0, e0);
    e1 = swap16_add(e1, e1);
    if ((lane & 24) == 0) *(unsigned*)((char*)lds + owr) = pack_bf16(e0, e1);
  }
  LDS_BARRIER();

  {
    const v4u oa0 = *(const v4u*)((const char*)lds + ord), oa1 = *(const v4u*)((const char*)lds + ord + 256);
#pragma unroll
    for (int j = 0; j < 8; ++j)
      dd[j] = __builtin_amdgcn_smfmac_f32_16x16x64_bf16(__builtin_bit_cast(bf16x8, oa0), __builtin_bit_cast(bf16x16, bt[j][0]), dd[j], sidx, 0, 0);
#pragma unroll
    for (int j = 0; j < 8; ++j)
      dd[j] = __builtin_amdgcn_smfmac_f32_16x16x64_bf16(__builtin_bit_cast(bf16x8, oa1), __builtin_bit_cast(bf16x16, bt[j][1]), dd[j], sidx, 0, 0);
    SB();
    float y[8];
#pragma unroll
    for (int j = 0; j < 8; ++j) y[j] = sel(lane & 8, dd[j][1] + dd[j][3], dd[j][0] + dd[j][2]);
    float u[2];
#pragma unroll
    for (int j = 0; j < 2; ++j) u[j] = sel(hh, sel(lane & 16, y[6 + j], y[4 + j]), sel(lane & 16, y[2 + j], y[j]));
    const float t0 = u[0] + dppf<XOR8>(u[0]), t1 = u[1] + dppf<XOR8>(u[1]);
    const float yv = (lane & 8) ? t1 : t0;
    __builtin_amdgcn_raw_buffer_store_b32((__float_as_uint(yv) & ~255u) | ytag, rw, yst, 0, AUX_SC1);
    ST(5);
  }
  if (w < 7 || me >= 14) { ST_FLUSH(); return; }

  {
    const unsigned yo = WS_Y + (grp * 14 + me) * 8192 + l16;
    const v4u tg = {tag & 255u, (tag >> 8) & 255u, (tag >> 16) & 255u, tag >> 24};
    const int ls = 32 * (me & 1) + 4 * c8 + 2 * hh + ((lane >> 4) & 1);
    GAS unsigned short* optr = (GAS unsigned short*)prm.out + b * (2 * DM) + blk * DM + 64 * (me >> 1) + 8 * (ls & 7) + (ls >> 3);
    __builtin_amdgcn_s_sleep(16);
    f32x4 sum;
    for (unsigned spins = 0;; ++spins) {
      v4u g[8];
#pragma unroll
      for (int t = 0; t < 8; ++t) g[t] = bld<AUX_SC1>(rw, yo, t * 1024);
      unsigned bad = 0;
      sum = f32x4{0.f, 0.f, 0.f, 0.f};
#pragma unroll
      for (int t = 0; t < 8; ++t) {
        const v4u x = g[t] ^ tg;
        bad |= (x[0] | x[1]) | (x[2] | x[3]);
        sum += __builtin_bit_cast(f32x4, x);
      }
      if (__all((bad & 255u) == 0) || spins > SPIN_CAP) break;
      asm volatile("" ::: "memory");
    }
    ST(6);
    float rr = swap16_add(swap32_add(sum[0], sum[2]), swap32_add(sum[1], sum[3]));
    rr += dppf<XOR8>(rr);
    if ((lane & 8) == 0) __hip_atomic_store(optr, (unsigned short)(pack_bf16(rr, rr) & 0xffffu), RLX_AGENT);
    if (me == 0 && b == 0 && lane == 0) __hip_atomic_store((GAS unsigned*)((GAS char*)prm.ws + WS_EPOCH + blk * 256), tag, RLX_AGENT);
    {
      const int el = (int)((unsigned)__builtin_amdgcn_s_memrealtime() - t_amp);
      const int tgt = 10000 + 5 * (el - 3800);
      while ((int)((unsigned)__builtin_amdgcn_s_memrealtime() - t_amp) < tgt) __builtin_amdgcn_s_sleep(1);
    }
    ST(7);
    ST_FLUSH();
  }
}

extern "C" void kernel_launch(void* const* d_in, const int* in_sizes, int n_in, void* d_out, int out_size, void* d_ws,
                              size_t ws_size, hipStream_t stream) {
  Params p;
  p.x[0] = d_in[0];
  p.k[0] = d_in[1];
  p.v[0] = d_in[2];
  p.wq[0] = d_in[3];
  p.wo[0] = d_in[4];
  p.x[1] = d_in[5];
  p.k[1] = d_in[6];
  p.v[1] = d_in[7];
  p.wq[1] = d_in[8];
  p.wo[1] = d_in[9];
  p.out = d_out;
  p.ws = d_ws;
  mha2_decode_kernel<<<dim3(256), dim3(NTHR), 0, stream>>>(p);
}
```

```cpp
#include <hip/hip_runtime.h>
#include <hip/hip_bf16.h>
#include <stdint.h>
#include <type_traits>

#define GAS __attribute__((address_space(1)))
#define CAS __attribute__((address_space(4)))
typedef unsigned v2u __attribute__((ext_vector_type(2)));
typedef unsigned v4u __attribute__((ext_vector_type(4)));
typedef unsigned v8u __attribute__((ext_vector_type(8)));
typedef float f32x2 __attribute__((ext_vector_type(2)));
typedef float f32x4 __attribute__((ext_vector_type(4)));
typedef __bf16 bf16x2 __attribute__((ext_vector_type(2)));
typedef __bf16 bf16x8 __attribute__((ext_vector_type(8)));
typedef __bf16 bf16x16 __attribute__((ext_vector_type(16)));
#define RLX_AGENT __ATOMIC_RELAXED, __HIP_MEMORY_SCOPE_AGENT

constexpr int WIN = 16;
constexpr int DIST = 6;
constexpr int FREE_N = 4;
constexpr int N_WQ = 2;
constexpr int N_QCALC = 6;
constexpr int N_QPOLL = 13;
constexpr int WM0 = 6, WMS = 2;
constexpr int WO0 = 12;
constexpr int N_ML = 96;
constexpr int G_ML = 123;
constexpr int PACE_T0 = -10;
constexpr int PACE_DUR = 3580;
constexpr int PACE_GAIN = 128;

constexpr int H = 16, SK = 4096, DH = 256, DM = 448;
constexpr int NTHR = 512;
constexpr int NLD = 64;
constexpr unsigned WS_EPOCH = 0;
constexpr unsigned WS_Q = 4096;
constexpr unsigned WS_ML = WS_Q + 65536;
constexpr unsigned WS_Y = WS_ML + 32768;
constexpr unsigned WS_END = WS_Y + 4 * 14 * 8192;
constexpr unsigned SPIN_CAP = 1u << 20;
constexpr unsigned OOB = 0x7fffff00u;
constexpr int AUX_NT = 2, AUX_SC1 = 16;

struct Params {
  const void* k[2];
  const void* wq[2];
  const void* x[2];
  void* ws;
  void* out;
  const void* v[2];
  const void* wo[2];
};

__device__ __forceinline__ float dot2(unsigned a, unsigned b, float c) {
  return __builtin_amdgcn_fdot2_f32_bf16(__builtin_bit_cast(bf16x2, a), __builtin_bit_cast(bf16x2, b), c, false);
}
__device__ __forceinline__ unsigned pack_bf16(float lo, float hi) {
  bf16x2 r;
  r.x = (__bf16)lo;
  r.y = (__bf16)hi;
  return __builtin_bit_cast(unsigned, r);
}
template <int CTRL> __device__ __forceinline__ float dppf(float x) {
  return __builtin_bit_cast(float, __builtin_amdgcn_mov_dpp(__builtin_bit_cast(int, x), CTRL, 0xf, 0xf, true));
}
constexpr int XOR1 = 0xB1, XOR2 = 0x4E, XOR7 = 0x141, XOR8 = 0x128, ROR4 = 0x124;
__device__ __forceinline__ float swap16_add(float a, float b) {
  auto r = __builtin_amdgcn_permlane16_swap(__float_as_uint(a), __float_as_uint(b), false, false);
  return __uint_as_float(r[0]) + __uint_as_float(r[1]);
}
__device__ __forceinline__ float swap32_add(float a, float b) {
  auto r = __builtin_amdgcn_permlane32_swap(__float_as_uint(a), __float_as_uint(b), false, false);
  return __uint_as_float(r[0]) + __uint_as_float(r[1]);
}
__device__ __forceinline__ float swap32_max(float a) {
  auto r = __builtin_amdgcn_permlane32_swap(__float_as_uint(a), __float_as_uint(a), false, false);
  return fmaxf(__uint_as_float(r[0]), __uint_as_float(r[1]));
}
__device__ __forceinline__ float half_max(float v) {
  v = fmaxf(v, dppf<XOR1>(v));
  v = fmaxf(v, dppf<XOR2>(v));
  v = fmaxf(v, dppf<XOR7>(v));
  v = fmaxf(v, dppf<XOR8>(v));
  auto r = __builtin_amdgcn_permlane16_swap(__float_as_uint(v), __float_as_uint(v), false, false);
  return fmaxf(__uint_as_float(r[0]), __uint_as_float(r[1]));
}
__device__ __forceinline__ float half_sum(float v) {
  v += dppf<XOR1>(v);
  v += dppf<XOR2>(v);
  v += dppf<XOR7>(v);
  v += dppf<XOR8>(v);
  return swap16_add(v, v);
}
__device__ __forceinline__ float sel(bool c, float a, float b) {
  asm volatile("" : "+v"(a), "+v"(b));
  return c ? a : b;
}
__device__ __forceinline__ bool tagged16(v4u g, unsigned tag) {
  return __builtin_amdgcn_perm(g[1], g[0], 0x07060302u) == tag && __builtin_amdgcn_perm(g[3], g[2], 0x07060302u) == tag;
}
template <int I, int N, class F> __device__ __forceinline__ void static_for(F&& f) {
  if constexpr (I < N) {
    f(std::integral_constant<int, I>{});
    static_for<I + 1, N>(f);
  }
}
#define LAMBDA(v) [&](auto v) __attribute__((always_inline))
typedef __amdgpu_buffer_rsrc_t rsrc_t;
__device__ __forceinline__ rsrc_t mk_rsrc(const void* base, unsigned bytes) {
  return __builtin_amdgcn_make_buffer_rsrc((void*)base, 0, bytes, 0x00020000);
}
template <int AUX> __device__ __forceinline__ v4u bld(rsrc_t rs, unsigned voff, unsigned soff) {
  return __builtin_amdgcn_raw_buffer_load_b128(rs, voff, soff, AUX);
}
#define SB() __builtin_amdgcn_sched_barrier(0)
#define LDS_BARRIER()                                  \
  do {                                                 \
    asm volatile("s_waitcnt lgkmcnt(0)" ::: "memory"); \
    __builtin_amdgcn_s_barrier();                      \
    asm volatile("" ::: "memory");                     \
    SB();                                              \
  } while (0)

constexpr int pace_thr(int n, int depth) {
  const int dith16 = 2 * (((n & 1) << 2) | (n & 2) | ((n & 4) >> 2)) + 1;
  const int lag = depth == 2   ? -50
                  : depth <= 4 ? PACE_GAIN * (16 * (depth - 2) - dith16) / 16
                               : 2 * PACE_GAIN + (PACE_GAIN + 300 * n / 128) * (16 - dith16) / 16;
  return PACE_T0 + n * PACE_DUR / 128 + lag;
}
template <int N> __device__ __forceinline__ void throttle(unsigned t_start) {
  const int el = (int)((unsigned)__builtin_amdgcn_s_memrealtime() - t_start);
  asm volatile(
      "s_waitcnt vmcnt(5)\n\ts_cmpk_ge_i32 %0, %1\n\ts_cbranch_scc1 1f\n\ts_waitcnt vmcnt(3)\n\t"
      "s_cmpk_ge_i32 %0, %2\n\ts_cbranch_scc1 1f\n\ts_waitcnt vmcnt(2)\n\t"
      "s_cmpk_ge_i32 %0, %3\n\ts_cbranch_scc1 1f\n\ts_waitcnt vmcnt(1)\n\t"
      "s_cmpk_ge_i32 %0, %4\n\ts_cbranch_scc1 1f\n\ts_waitcnt vmcnt(0)\n1:" ::"s"(el),
      "i"(pace_thr(N, 5)), "i"(pace_thr(N, 4)), "i"(pace_thr(N, 3)), "i"(pace_thr(N, 2))
      : "scc");
}
constexpr int issue_after(int g) {
  const int n = g < 2 * (WIN - DIST) ? ((g & 1) ? WIN + g / 2 : -1) : g + DIST;
  return n < 2 * NLD ? n : -1;
}

constexpr int L_QP = 0;
constexpr int L_P = L_QP + 8 * 64;
constexpr int L_ACC = L_P + 8 * 128;
constexpr int L_O = L_ACC + 8 * 256;
constexpr int L_ML = L_O + 128;
constexpr int L_CNT = L_ML + 16;
constexpr int L_END = L_CNT + 4;

__global__ void __launch_bounds__(NTHR, 2) mha2_decode_kernel(Params prm) {
  __shared__ __attribute__((aligned(16))) float lds[L_END];
  const int tid = threadIdx.x, lane = tid & 63;
  const int w = __builtin_amdgcn_readfirstlane(tid >> 6);
  const int bid = blockIdx.x;
  const int team = (bid & 7) * 4 + (bid >> 6);
  const int blk = team >> 4, h = team & 15;
  const int r = (bid >> 3) & 7, b = r >> 2, s = r & 3;
  const int hh = lane >> 5, l32 = lane & 31, rc = lane >> 3, c8 = lane & 7;
  const int grp = blk * 2 + b, me = s * 16 + h;
  const char* const pk = (const char*)(blk ? prm.k[1] : prm.k[0]);
  const char* const pv = (const char*)(blk ? prm.v[1] : prm.v[0]);
  const char* const pwq = (const char*)(blk ? prm.wq[1] : prm.wq[0]);
  const char* const pwo = (const char*)(blk ? prm.wo[1] : prm.wo[0]);
  const char* const px = (const char*)(blk ? prm.x[1] : prm.x[0]);

  const size_t kvoff = (size_t)(b * H + h) * SK * DH * 2;
  const rsrc_t rk = mk_rsrc(pk + kvoff, SK * DH * 2);
  const rsrc_t rv = mk_rsrc(pv + kvoff, SK * DH * 2);
  unsigned kvb = (unsigned)(grp * 16 + h) * 32768u + ((w & 1) << 9) + ((w & 6) << 11) + ((s & 1) << 14) + ((s & 2) << 15);
  unsigned kvo = 0;
  const unsigned l16 = lane * 16;
  const rsrc_t rw = mk_rsrc(prm.ws, WS_END);
  const rsrc_t rq = mk_rsrc(pwq + (size_t)(w * 56) * (H * DH * 2) + h * (DH * 2) + r * 64, 55 * (H * DH * 2) + 64);
  const rsrc_t rx = mk_rsrc(px + w * 112, DM * 2 + 112);
  const rsrc_t ro = mk_rsrc(pwo + (size_t)h * DH * (DM * 2) + w * 128, w < 7 ? 255 * (DM * 2) + 128 : 0);

  unsigned tag, t_start;
  v4u ring[WIN];
  v4u wqv[4], qg, ml;
  v2u x0, x1;
  v4u wb[32];
  v8u bt[8][2];

  auto q_share = [&]() __attribute__((always_inline)) {
    float a0[8], a1[8];
#pragma unroll
    for (int j = 0; j < 4; ++j) {
      const unsigned p0 = __builtin_amdgcn_perm(wqv[1][j], wqv[0][j], 0x05040100u), p1 = __builtin_amdgcn_perm(wqv[1][j], wqv[0][j], 0x07060302u);
      const unsigned p2 = __builtin_amdgcn_perm(wqv[3][j], wqv[2][j], 0x05040100u), p3 = __builtin_amdgcn_perm(wqv[3][j], wqv[2][j], 0x07060302u);
      a0[2 * j] = dot2(p2, x0[1], dot2(p0, x0[0], 0.f));
      a0[2 * j + 1] = dot2(p3, x0[1], dot2(p1, x0[0], 0.f));
      a1[2 * j] = dot2(p2, x1[1], dot2(p0, x1[0], 0.f));
      a1[2 * j + 1] = dot2(p3, x1[1], dot2(p1, x1[0], 0.f));
    }
    float z[8];
#pragma unroll
    for (int j = 0; j < 8; ++j) z[j] = swap32_add(a0[j], a1[j]);
    f32x4 u;
#pragma unroll
    for (int j = 0; j < 4; ++j) {
      float t = swap16_add(z[j], z[j + 4]);
      t += dppf<ROR4>(t);
      t += dppf<XOR8>(t);
      u[j] = t;
    }
    if ((lane & 12) == 0) *(f32x4*)(lds + L_QP + w * 64 + hh * 32 + (lane & 3) * 8 + ((lane >> 4) & 1) * 4) = u;
    unsigned arrived = 0;
    asm volatile("s_waitcnt lgkmcnt(0)" : "+s"(tag)::"memory");
    if (lane == 0) arrived = __hip_atomic_fetch_add((unsigned*)lds + L_CNT, 1u, __ATOMIC_RELAXED, __HIP_MEMORY_SCOPE_WORKGROUP);
    asm volatile("" ::: "memory");
    if (__builtin_amdgcn_readfirstlane(arrived) == 7) {
      float q = 0.f;
#pragma unroll
      for (int i = 0; i < 8; ++i) q += lds[L_QP + i * 64 + lane];
      __builtin_amdgcn_raw_buffer_store_b32((pack_bf16(q, q) & 0xffffu) | (((lane & 1) ? tag : tag << 16) & 0xffff0000u), rw,
                                            WS_Q + (team * 2 + hh) * 1024 + ((lane >> 2) & 1) * 512 + (r * 4 + (l32 >> 3)) * 16 + (lane & 3) * 4, 0, AUX_SC1);
    }
    SB();
  };

  auto issue = LAMBDA(nc) {
    constexpr int n = decltype(nc)::value;
    if constexpr (n == N_WQ) {
      t_start = (unsigned)__builtin_amdgcn_s_memrealtime();
      if (tid < 2) ((unsigned*)lds)[L_CNT + tid] = 0u;
      LDS_BARRIER();
      tag = *(const CAS unsigned*)((const CAS char*)prm.ws + WS_EPOCH + blk * 256) + 0x01010101u;
      const unsigned xoff = lane < 56 ? (lane >> 2) * 8 : OOB;
      x0 = __builtin_amdgcn_raw_buffer_load_b64(rx, xoff, 0, 0);
      x1 = __builtin_amdgcn_raw_buffer_load_b64(rx, xoff, DM * 2, 0);
#pragma unroll
      for (int i = 0; i < 4; ++i) wqv[i] = bld<0>(rq, (lane >> 2) * 4 * (H * DH * 2) + (lane & 3) * 16, i * (H * DH * 2));
    }
    if constexpr (n == N_QCALC) q_share();
    if constexpr (n >= FREE_N) throttle<n>(t_start);
    if constexpr (n == N_QPOLL) qg = bld<AUX_SC1>(rw, WS_Q + (team * 2 + b) * 1024 + l16, 0);
    if constexpr (n == N_ML) ml = bld<AUX_SC1>(rw, WS_ML + (grp * 16 + h) * 512 + l32 * 16, 0);
    SB();
    if constexpr ((n & 1) == 0) {
      asm volatile("" : "+s"(kvb));
      kvo = (kvb + ((n & 2) << 14) + ((n & 60) << 15)) & (SK * DH * 2 - 1);
    }
    ring[n % WIN] = bld<AUX_NT>(n < NLD ? rk : rv, l32 * 16 + hh * 2048 + (n & 1) * 1024, kvo);
    SB();
    static_for<0, 32>(LAMBDA(jc) {
      constexpr int j = decltype(jc)::value;
      if constexpr (n == (j < 4 ? WM0 + j * WMS : WO0 + 3 * j)) {
        wb[j] = bld<0>(ro, 32 * rc * (DM * 2) + c8 * 16 + (j & 3) * (DM * 2), (unsigned)(((j >> 2) ^ r) * 4 * (DM * 2)));
        SB();
      }
    });
    constexpr int P0 = WO0 + DIST + 1;
    static_assert(P0 + 31 * 3 < 2 * NLD, "all wo rows are paired inside the stream");
    if constexpr (n >= P0 && (n - P0) % 3 == 0 && (n - P0) / 3 < 32 && (((n - P0) / 3) & 1)) {
      constexpr int j = (n - P0) / 3, t = j >> 1;
#pragma unroll
      for (int jj = 0; jj < 4; ++jj) {
        unsigned lo = __builtin_amdgcn_perm(wb[2 * t + 1][jj], wb[2 * t][jj], 0x05040100u);
        unsigned hi = __builtin_amdgcn_perm(wb[2 * t + 1][jj], wb[2 * t][jj], 0x07060302u);
        asm volatile("" : "+v"(lo), "+v"(hi));
        bt[2 * jj][t >> 3][t & 7] = lo;
        bt[2 * jj + 1][t >> 3][t & 7] = hi;
      }
      if constexpr (j == 31) {
#pragma unroll
        for (int jj = 0; jj < 8; ++jj) asm volatile("" : "+v"(bt[jj][0]), "+v"(bt[jj][1]));
      }
      SB();
    }
  };
  auto after = LAMBDA(gc) {
    constexpr int n = issue_after(decltype(gc)::value);
    SB();
    if constexpr (n >= 0) issue(std::integral_constant<int, n>{});
  };

  static_for<0, WIN>(issue);
  if (__builtin_expect(!__all(tagged16(qg, tag)), 0)) {
    for (unsigned spins = 0; spins < SPIN_CAP; ++spins) {
      asm volatile("" ::: "memory");
      qg = bld<AUX_SC1>(rw, WS_Q + (team * 2 + b) * 1024 + l16, 0);
      if (__all(tagged16(qg, tag))) break;
    }
  }
  v4u qv;
  {
    const unsigned q0 = __builtin_amdgcn_perm(qg[1], qg[0], 0x05040100u), q1 = __builtin_amdgcn_perm(qg[3], qg[2], 0x05040100u);
    auto e = __builtin_amdgcn_permlane32_swap(q0, q0, false, false);
    auto o = __builtin_amdgcn_permlane32_swap(q1, q1, false, false);
    qv = v4u{e[0], o[0], e[1], o[1]};
  }

  float sc[2];
  {
    float xs[16];
    float red[2];
    static_for<0, NLD>(LAMBDA(tc) {
      constexpr int t = decltype(tc)::value;
      const v4u cur = ring[t % WIN];
      xs[t & 15] = dot2(cur[3], qv[3], dot2(cur[2], qv[2], dot2(cur[1], qv[1], dot2(cur[0], qv[0], 0.f))));
      asm volatile("" : "+v"(xs[t & 15]));
      after(tc);
      if constexpr ((t & 15) == 15) {
#define TR_STEP(HALF, CTRL)                                               \
  _Pragma("unroll") for (int u = 0; u < (HALF); ++u) {                    \
    const float lo_ = xs[u], hi_ = xs[u + (HALF)];                        \
    const float t1_ = lo_ + dppf<CTRL>(lo_), t2_ = hi_ + dppf<CTRL>(hi_); \
    xs[u] = (lane & (HALF)) ? t2_ : t1_;                                  \
  }
        TR_STEP(8, XOR8) TR_STEP(4, XOR7) TR_STEP(2, XOR2) TR_STEP(1, XOR1)
#undef TR_STEP
        asm volatile("" : "+v"(xs[0]));
        red[(t >> 4) & 1] = xs[0];
        if constexpr ((t & 31) == 31) sc[t >> 5] = swap16_add(red[0], red[1]);
        SB();
      }
    });
  }
  constexpr float C2 = 0.0625f * 1.4426950408889634f;
  const float mw = swap32_max(half_max(fmaxf(sc[0], sc[1]))) * C2;
  {
    const float p0 = __builtin_amdgcn_exp2f(fmaf(sc[0], C2, -mw));
    const float p1 = __builtin_amdgcn_exp2f(fmaf(sc[1], C2, -mw));
    lds[L_P + w * 128 + hh * 64 + l32] = p0;
    lds[L_P + w * 128 + hh * 64 + 32 + l32] = p1;
    float lw = half_sum(p0 + p1);
    lw = swap32_add(lw, lw);
    unsigned arrived = 0;
    if (lane == 0) {
      *(f32x2*)(lds + L_ML + w * 2) = f32x2{mw, lw};
      asm volatile("" ::: "memory");
      arrived = __hip_atomic_fetch_add((unsigned*)lds + L_CNT + 1, 1u, __ATOMIC_RELAXED, __HIP_MEMORY_SCOPE_WORKGROUP);
    }
    asm volatile("" ::: "memory");
    if (__builtin_amdgcn_readfirstlane(arrived) == 7) {
      const v2u e = *(const v2u*)(lds + L_ML + c8 * 2);
      __builtin_amdgcn_raw_buffer_store_b128(v4u{__builtin_amdgcn_perm(tag, e[0], 0x05040100u), __builtin_amdgcn_perm(tag, e[0], 0x07060302u),
                                                 __builtin_amdgcn_perm(tag, e[1], 0x05040100u), __builtin_amdgcn_perm(tag, e[1], 0x07060302u)}, rw,
                                             (lane < 8 ? WS_ML : OOB) + (grp * 16 + h) * 512 + (s * 8 + c8) * 16, 0, AUX_SC1);
    }
  }
  unsigned yst = (w < 7 ? WS_Y : OOB) + ((grp * 14 + w * 2 + hh) * 64 + me) * 128 + l32 * 4;
  unsigned ytag = (tag >> ((lane & 3) * 8)) & 255u;
  unsigned accrd = L_ACC * 4 + rc * 1024 + w * 128 + c8 * 16;
  const int kk = c8 ^ r;
  unsigned owr = L_O * 4 + (((kk >> 2) * 4 + ((kk >> 1) & 1) * 2 + (w >> 2)) * 16 + hh * 8 + (w & 1) * 4 + ((w >> 1) & 1) * 2 + (kk & 1)) * 4;
  asm volatile("s_waitcnt lgkmcnt(0)" : "+v"(yst), "+v"(ytag), "+v"(accrd), "+v"(owr)::"memory");
  SB();

  f32x2 acc[4];
#pragma unroll
  for (int j = 0; j < 4; ++j) acc[j] = f32x2{0.f, 0.f};
  float fw;
  bool mlok;
  auto ml_eval = [&]() __attribute__((always_inline)) {
    mlok = __all(tagged16(ml, tag));
    const float m = __uint_as_float(__builtin_amdgcn_perm(ml[1], ml[0], 0x05040100u));
    const float M = half_max(m);
    const float L = half_sum(__builtin_amdgcn_exp2f(m - M) * __uint_as_float(__builtin_amdgcn_perm(ml[3], ml[2], 0x05040100u)));
    fw = __builtin_amdgcn_exp2f(mw - M) / L;
  };
  {
    const f32x4* pl = (const f32x4*)(lds + L_P + w * 128 + hh * 64);
    f32x4 pc = pl[0];
    static_for<0, NLD>(LAMBDA(tc) {
      constexpr int t = decltype(tc)::value;
      const v4u cur = ring[(t + NLD) % WIN];
      const float pv_ = pc[t & 3];
      if constexpr ((t & 3) == 3 && t + 1 < NLD) pc = pl[(t + 1) >> 2];
#pragma unroll
      for (int j = 0; j < 4; ++j) {
        const f32x2 vv = {__uint_as_float(cur[j] << 16), __uint_as_float(cur[j] & 0xffff0000u)};
        acc[j] = __builtin_elementwise_fma(vv, f32x2{pv_, pv_}, acc[j]);
      }
      asm volatile("" : "+v"(acc[0]), "+v"(acc[1]), "+v"(acc[2]), "+v"(acc[3]));
      after(std::integral_constant<int, t + NLD>{});
      if constexpr (t + NLD == G_ML) {
        ml_eval();
        asm volatile("" : "+v"(fw));
        SB();
      }
    });
  }
  if (__builtin_expect(!mlok, 0)) {
    for (unsigned spins = 0; !mlok && spins < SPIN_CAP; ++spins) {
      __builtin_amdgcn_s_sleep(8);
      asm volatile("" ::: "memory");
      ml = bld<AUX_SC1>(rw, WS_ML + (grp * 16 + h) * 512 + l32 * 16, 0);
      ml_eval();
    }
  }

  {
    f32x4 rr;
#pragma unroll
    for (int j = 0; j < 4; ++j) rr[j] = swap32_add(acc[j >> 1][j & 1], acc[2 + (j >> 1)][j & 1]) * fw;
    *(f32x4*)(lds + L_ACC + w * 256 + l32 * 8 + hh * 4) = rr;
  }
  LDS_BARRIER();
  const f32x4 a = *(const f32x4*)((const char*)lds + accrd);
  SB();
  int sidx = (lane & 2) ? (int)0xEEEEEEEEu : 0x44444444;
  unsigned ord = L_O * 4 + (lane >> 4) * 64 + (lane & 3) * 16;
  f32x4 dd[8];
#pragma unroll
  for (int j = 0; j < 8; ++j) {
    dd[j] = f32x4{0.f, 0.f, 0.f, 0.f};
    asm volatile("" : "+v"(dd[j]));
  }
  asm volatile("" : "+v"(sidx), "+v"(ord));
  SB();
  {
    float e0 = swap32_add(a[0], a[2]), e1 = swap32_add(a[1], a[3]);
    e0 += dppf<XOR8>(e0);
    e1 += dppf<XOR8>(e1);
    e0 = swap16_add(e0, e0);
    e1 = swap16_add(e1, e1);
    if ((lane & 24) == 0) *(unsigned*)((char*)lds + owr) = pack_bf16(e0, e1);
  }
  LDS_BARRIER();

  {
    const v4u oa0 = *(const v4u*)((const char*)lds + ord), oa1 = *(const v4u*)((const char*)lds + ord + 256);
#pragma unroll
    for (int j = 0; j < 8; ++j)
      dd[j] = __builtin_amdgcn_smfmac_f32_16x16x64_bf16(__builtin_bit_cast(bf16x8, oa0), __builtin_bit_cast(bf16x16, bt[j][0]), dd[j], sidx, 0, 0);
#pragma unroll
    for (int j = 0; j < 8; ++j)
      dd[j] = __builtin_amdgcn_smfmac_f32_16x16x64_bf16(__builtin_bit_cast(bf16x8, oa1), __builtin_bit_cast(bf16x16, bt[j][1]), dd[j], sidx, 0, 0);
    SB();
    float y[8];
#pragma unroll
    for (int j = 0; j < 8; ++j) y[j] = sel(lane & 8, dd[j][1] + dd[j][3], dd[j][0] + dd[j][2]);
    float u[2];
#pragma unroll
    for (int j = 0; j < 2; ++j) u[j] = sel(hh, sel(lane & 16, y[6 + j], y[4 + j]), sel(lane & 16, y[2 + j], y[j]));
    const float t0 = u[0] + dppf<XOR8>(u[0]), t1 = u[1] + dppf<XOR8>(u[1]);
    const float yv = (lane & 8) ? t1 : t0;
    __builtin_amdgcn_raw_buffer_store_b32((__float_as_uint(yv) & ~255u) | ytag, rw, yst, 0, AUX_SC1);
  }
  if (w < 7 || me >= 14) return;

  {
    const unsigned yo = WS_Y + (grp * 14 + me) * 8192 + l16;
    const v4u tg = {tag & 255u, (tag >> 8) & 255u, (tag >> 16) & 255u, tag >> 24};
    const int ls = 32 * (me & 1) + 4 * c8 + 2 * hh + ((lane >> 4) & 1);
    GAS unsigned short* optr = (GAS unsigned short*)prm.out + b * (2 * DM) + blk * DM + 64 * (me >> 1) + 8 * (ls & 7) + (ls >> 3);
    __builtin_amdgcn_s_sleep(16);
    f32x4 sum;
    for (unsigned spins = 0;; ++spins) {
      v4u g[8];
#pragma unroll
      for (int t = 0; t < 8; ++t) g[t] = bld<AUX_SC1>(rw, yo, t * 1024);
      unsigned bad = 0;
      sum = f32x4{0.f, 0.f, 0.f, 0.f};
#pragma unroll
      for (int t = 0; t < 8; ++t) {
        const v4u x = g[t] ^ tg;
        bad |= (x[0] | x[1]) | (x[2] | x[3]);
        sum += __builtin_bit_cast(f32x4, x);
      }
      if (__all((bad & 255u) == 0) || spins > SPIN_CAP) break;
      asm volatile("" ::: "memory");
    }
    float rr = swap16_add(swap32_add(sum[0], sum[2]), swap32_add(sum[1], sum[3]));
    rr += dppf<XOR8>(rr);
    if ((lane & 8) == 0) __hip_atomic_store(optr, (unsigned short)(pack_bf16(rr, rr) & 0xffffu), RLX_AGENT);
    if (me == 0 && b == 0 && lane == 0) __hip_atomic_store((GAS unsigned*)((GAS char*)prm.ws + WS_EPOCH + blk * 256), tag, RLX_AGENT);
  }
}

extern "C" void kernel_launch(void* const* d_in, const int* in_sizes, int n_in, void* d_out, int out_size, void* d_ws,
                              size_t ws_size, hipStream_t stream) {
  Params p;
  p.x[0] = d_in[0];
  p.k[0] = d_in[1];
  p.v[0] = d_in[2];
  p.wq[0] = d_in[3];
  p.wo[0] = d_in[4];
  p.x[1] = d_in[5];
  p.k[1] = d_in[6];
  p.v[1] = d_in[7];
  p.wq[1] = d_in[8];
  p.wo[1] = d_in[9];
  p.out = d_out;
  p.ws = d_ws;
  mha2_decode_kernel<<<dim3(256), dim3(NTHR), 0, stream>>>(p);
}
```

```cpp
#define AMP
#define P_T0 -10
#define ZM 1
#include <hip/hip_runtime.h>
#include <hip/hip_bf16.h>
#include <stdint.h>
#include <type_traits>

#define GAS __attribute__((address_space(1)))
#define CAS __attribute__((address_space(4)))
typedef unsigned v2u __attribute__((ext_vector_type(2)));
typedef unsigned v4u __attribute__((ext_vector_type(4)));
typedef unsigned v8u __attribute__((ext_vector_type(8)));
typedef float f32x2 __attribute__((ext_vector_type(2)));
typedef float f32x4 __attribute__((ext_vector_type(4)));
typedef __bf16 bf16x2 __attribute__((ext_vector_type(2)));
typedef __bf16 bf16x8 __attribute__((ext_vector_type(8)));
typedef __bf16 bf16x16 __attribute__((ext_vector_type(16)));
#define RLX_AGENT __ATOMIC_RELAXED, __HIP_MEMORY_SCOPE_AGENT

constexpr int WIN = 16;
constexpr int DIST = 6;
constexpr int FREE_N = 4;
constexpr int N_WQ = 2;
constexpr int N_QCALC = 6;
constexpr int N_QPOLL = 13;
constexpr int WM0 = 6, WMS = 2;
constexpr int WO0 = 12;
constexpr int N_ML = 96;
constexpr int G_ML = 123;
constexpr int PACE_T0 = P_T0;
constexpr int PACE_DUR = 3560;
constexpr int PACE_GAIN = 128;

constexpr int H = 16, SK = 4096, DH = 256, DM = 448;
constexpr int NTHR = 512;
constexpr int NLD = 64;
constexpr unsigned WS_EPOCH = 0;
constexpr unsigned WS_Q = 4096;
constexpr unsigned WS_ML = WS_Q + 65536;
constexpr unsigned WS_Y = WS_ML + 32768;
constexpr unsigned WS_END = WS_Y + 4 * 14 * 8192;
constexpr unsigned SPIN_CAP = 1u << 20;
constexpr unsigned OOB = 0x7fffff00u;
constexpr int AUX_NT = 2, AUX_SC1 = 16;

struct Params {
  const void* k[2];
  const void* wq[2];
  const void* x[2];
  void* ws;
  void* out;
  const void* v[2];
  const void* wo[2];
};

__device__ __forceinline__ float dot2(unsigned a, unsigned b, float c) {
  return __builtin_amdgcn_fdot2_f32_bf16(__builtin_bit_cast(bf16x2, a), __builtin_bit_cast(bf16x2, b), c, false);
}
__device__ __forceinline__ unsigned pack_bf16(float lo, float hi) {
  bf16x2 r;
  r.x = (__bf16)lo;
  r.y = (__bf16)hi;
  return __builtin_bit_cast(unsigned, r);
}
template <int CTRL> __device__ __forceinline__ float dppf(float x) {
  return __builtin_bit_cast(float, __builtin_amdgcn_mov_dpp(__builtin_bit_cast(int, x), CTRL, 0xf, 0xf, true));
}
constexpr int XOR1 = 0xB1, XOR2 = 0x4E, XOR7 = 0x141, XOR8 = 0x128, ROR4 = 0x124;
__device__ __forceinline__ float swap16_add(float a, float b) {
  auto r = __builtin_amdgcn_permlane16_swap(__float_as_uint(a), __float_as_uint(b), false, false);
  return __uint_as_float(r[0]) + __uint_as_float(r[1]);
}
__device__ __forceinline__ float swap32_add(float a, float b) {
  auto r = __builtin_amdgcn_permlane32_swap(__float_as_uint(a), __float_as_uint(b), false, false);
  return __uint_as_float(r[0]) + __uint_as_float(r[1]);
}
__device__ __forceinline__ float swap32_max(float a) {
  auto r = __builtin_amdgcn_permlane32_swap(__float_as_uint(a), __float_as_uint(a), false, false);
  return fmaxf(__uint_as_float(r[0]), __uint_as_float(r[1]));
}
__device__ __forceinline__ float half_max(float v) {
  v = fmaxf(v, dppf<XOR1>(v));
  v = fmaxf(v, dppf<XOR2>(v));
  v = fmaxf(v, dppf<XOR7>(v));
  v = fmaxf(v, dppf<XOR8>(v));
  auto r = __builtin_amdgcn_permlane16_swap(__float_as_uint(v), __float_as_uint(v), false, false);
  return fmaxf(__uint_as_float(r[0]), __uint_as_float(r[1]));
}
__device__ __forceinline__ float half_sum(float v) {
  v += dppf<XOR1>(v);
  v += dppf<XOR2>(v);
  v += dppf<XOR7>(v);
  v += dppf<XOR8>(v);
  return swap16_add(v, v);
}
__device__ __forceinline__ float sel(bool c, float a, float b) {
  asm volatile("" : "+v"(a), "+v"(b));
  return c ? a : b;
}
__device__ __forceinline__ bool tagged16(v4u g, unsigned tag) {
  return __builtin_amdgcn_perm(g[1], g[0], 0x07060302u) == tag && __builtin_amdgcn_perm(g[3], g[2], 0x07060302u) == tag;
}
template <int I, int N, class F> __device__ __forceinline__ void static_for(F&& f) {
  if constexpr (I < N) {
    f(std::integral_constant<int, I>{});
    static_for<I + 1, N>(f);
  }
}
#define LAMBDA(v) [&](auto v) __attribute__((always_inline))
typedef __amdgpu_buffer_rsrc_t rsrc_t;
__device__ __forceinline__ rsrc_t mk_rsrc(const void* base, unsigned bytes) {
  return __builtin_amdgcn_make_buffer_rsrc((void*)base, 0, bytes, 0x00020000);
}
template <int AUX> __device__ __forceinline__ v4u bld(rsrc_t rs, unsigned voff, unsigned soff) {
  return __builtin_amdgcn_raw_buffer_load_b128(rs, voff, soff, AUX);
}
#define SB() __builtin_amdgcn_sched_barrier(0)
#define LDS_BARRIER()                                  \
  do {                                                 \
    asm volatile("s_waitcnt lgkmcnt(0)" ::: "memory"); \
    __builtin_amdgcn_s_barrier();                      \
    asm volatile("" ::: "memory");                     \
    SB();                                              \
  } while (0)

constexpr int pace_thr(int n, int depth) {
  const int dith16 = 2 * (((n & 1) << 2) | (n & 2) | ((n & 4) >> 2)) + 1;
  const int lag = depth == 2   ? -50
                  : depth <= 4 ? PACE_GAIN * (16 * (depth - 2) - dith16) / 16
                               : 2 * PACE_GAIN + (PACE_GAIN + 300 * n / 128) * (16 - dith16) / 16;
  return PACE_T0 + n * PACE_DUR / 128 + lag;
}
template <int N> __device__ __forceinline__ void throttle(unsigned t_start) {
  const int el = (int)((unsigned)__builtin_amdgcn_s_memrealtime() - t_start);
  asm volatile(
      "s_waitcnt vmcnt(5)\n\ts_cmpk_ge_i32 %0, %1\n\ts_cbranch_scc1 1f\n\ts_waitcnt vmcnt(3)\n\t"
      "s_cmpk_ge_i32 %0, %2\n\ts_cbranch_scc1 1f\n\ts_waitcnt vmcnt(2)\n\t"
      "s_cmpk_ge_i32 %0, %3\n\ts_cbranch_scc1 1f\n\ts_waitcnt vmcnt(1)\n\t"
      "s_cmpk_ge_i32 %0, %4\n\ts_cbranch_scc1 1f\n\ts_waitcnt vmcnt(0)\n1:" ::"s"(el),
      "i"(pace_thr(N, 5)), "i"(pace_thr(N, 4)), "i"(pace_thr(N, 3)), "i"(pace_thr(N, 2))
      : "scc");
}
constexpr int issue_after(int g) {
  const int n = g < 2 * (WIN - DIST) ? ((g & 1) ? WIN + g / 2 : -1) : g + DIST;
  return n < 2 * NLD ? n : -1;
}

#define ST(k)
#define ST_FLUSH()

constexpr int L_QP = 0;
constexpr int L_P = L_QP + 8 * 64;
constexpr int L_ACC = L_P + 8 * 128;
constexpr int L_O = L_ACC + 8 * 256;
constexpr int L_ML = L_O + 128;
constexpr int L_CNT = L_ML + 16;
constexpr int L_END = L_CNT + 4;

__global__ void __launch_bounds__(NTHR, 2) mha2_decode_kernel(Params prm) {
  __shared__ __attribute__((aligned(16))) float lds[L_END];
  ST(0);
  const unsigned t_amp = (unsigned)__builtin_amdgcn_s_memrealtime();
  SB();
  asm volatile("" ::"s"(prm.k[0]), "s"(t_amp));
  SB();
  const unsigned t_k = (unsigned)__builtin_amdgcn_s_memrealtime();
  asm volatile("" ::"s"(t_k));
  SB();
  unsigned t_start;
  const int tid = threadIdx.x, lane = tid & 63;
  const int w = __builtin_amdgcn_readfirstlane(tid >> 6);
  const int bid = blockIdx.x;
  const int team = (bid & 7) * 4 + (bid >> 6);
  const int blk = team >> 4, h = team & 15;
  const int r = (bid >> 3) & 7, b = r >> 2, s = r & 3;
  const int hh = lane >> 5, l32 = lane & 31, rc = lane >> 3, c8 = lane & 7;
  const int grp = blk * 2 + b, me = s * 16 + h;
  const char* const pk = (const char*)(blk ? prm.k[1] : prm.k[0]);
  const char* const pv = (const char*)(blk ? prm.v[1] : prm.v[0]);
  const char* const pwq = (const char*)(blk ? prm.wq[1] : prm.wq[0]);
  const char* const pwo = (const char*)(blk ? prm.wo[1] : prm.wo[0]);
  const char* const px = (const char*)(blk ? prm.x[1] : prm.x[0]);

  const size_t kvoff = (size_t)(b * H + h) * SK * DH * 2;
  const rsrc_t rk = mk_rsrc(pk + kvoff, SK * DH * 2);
  const rsrc_t rv = mk_rsrc(pv + kvoff, SK * DH * 2);
  unsigned kvb = (unsigned)(grp * 16 + h) * 32768u + ((w & 1) << 9) + ((w & 6) << 11) + ((s & 1) << 14) + ((s & 2) << 15);
  unsigned kvo = 0;
  const unsigned l16 = lane * 16;
  const rsrc_t rw = mk_rsrc(prm.ws, WS_END);
  const rsrc_t rq = mk_rsrc(pwq + (size_t)(w * 56) * (H * DH * 2) + h * (DH * 2) + r * 64, 55 * (H * DH * 2) + 64);
  const rsrc_t rx = mk_rsrc(px + w * 112, DM * 2 + 112);
  const rsrc_t ro = mk_rsrc(pwo + (size_t)h * DH * (DM * 2) + w * 128, w < 7 ? 255 * (DM * 2) + 128 : 0);

  unsigned tag;
  v4u ring[WIN];
  v4u wqv[4], qg, ml;
  v2u x0, x1;
  v4u wb[32];
  v8u bt[8][2];

  auto q_share = [&]() __attribute__((always_inline)) {
    float a0[8], a1[8];
#pragma unroll
    for (int j = 0; j < 4; ++j) {
      const unsigned p0 = __builtin_amdgcn_perm(wqv[1][j], wqv[0][j], 0x05040100u), p1 = __builtin_amdgcn_perm(wqv[1][j], wqv[0][j], 0x07060302u);
      const unsigned p2 = __builtin_amdgcn_perm(wqv[3][j], wqv[2][j], 0x05040100u), p3 = __builtin_amdgcn_perm(wqv[3][j], wqv[2][j], 0x07060302u);
      a0[2 * j] = dot2(p2, x0[1], dot2(p0, x0[0], 0.f));
      a0[2 * j + 1] = dot2(p3, x0[1], dot2(p1, x0[0], 0.f));
      a1[2 * j] = dot2(p2, x1[1], dot2(p0, x1[0], 0.f));
      a1[2 * j + 1] = dot2(p3, x1[1], dot2(p1, x1[0], 0.f));
    }
    float z[8];
#pragma unroll
    for (int j = 0; j < 8; ++j) z[j] = swap32_add(a0[j], a1[j]);
    f32x4 u;
#pragma unroll
    for (int j = 0; j < 4; ++j) {
      float t = swap16_add(z[j], z[j + 4]);
      t += dppf<ROR4>(t);
      t += dppf<XOR8>(t);
      u[j] = t;
    }
    if ((lane & 12) == 0) *(f32x4*)(lds + L_QP + w * 64 + hh * 32 + (lane & 3) * 8 + ((lane >> 4) & 1) * 4) = u;
    ST(1);
    unsigned arrived = 0;
    asm volatile("s_waitcnt lgkmcnt(0)" : "+s"(tag)::"memory");
    if (lane == 0) arrived = __hip_atomic_fetch_add((unsigned*)lds + L_CNT, 1u, __ATOMIC_RELAXED, __HIP_MEMORY_SCOPE_WORKGROUP);
    asm volatile("" ::: "memory");
    if (__builtin_amdgcn_readfirstlane(arrived) == 7) {
      float q = 0.f;
#pragma unroll
      for (int i = 0; i < 8; ++i) q += lds[L_QP + i * 64 + lane];
      __builtin_amdgcn_raw_buffer_store_b32((pack_bf16(q, q) & 0xffffu) | (((lane & 1) ? tag : tag << 16) & 0xffff0000u), rw,
                                            WS_Q + (team * 2 + hh) * 1024 + ((lane >> 2) & 1) * 512 + (r * 4 + (l32 >> 3)) * 16 + (lane & 3) * 4, 0, AUX_SC1);
    }
    SB();
  };

  auto issue = LAMBDA(nc) {
    constexpr int n = decltype(nc)::value;
    if constexpr (n == N_WQ) {
      t_start = (unsigned)__builtin_amdgcn_s_memrealtime();
      if (tid < 2) ((unsigned*)lds)[L_CNT + tid] = 0u;
      LDS_BARRIER();
      tag = *(const CAS unsigned*)((const CAS char*)prm.ws + WS_EPOCH + blk * 256) + 0x01010101u;
      const unsigned xoff = lane < 56 ? (lane >> 2) * 8 : OOB;
      x0 = __builtin_amdgcn_raw_buffer_load_b64(rx, xoff, 0, 0);
      x1 = __builtin_amdgcn_raw_buffer_load_b64(rx, xoff, DM * 2, 0);
#pragma unroll
      for (int i = 0; i < 4; ++i) wqv[i] = bld<0>(rq, (lane >> 2) * 4 * (H * DH * 2) + (lane & 3) * 16, i * (H * DH * 2));
    }
    if constexpr (n == N_QCALC) q_share();
    if constexpr (n >= FREE_N) throttle<n>(t_start);
    if constexpr (n == N_QPOLL) qg = bld<AUX_SC1>(rw, WS_Q + (team * 2 + b) * 1024 + l16, 0);
    if constexpr (n == N_ML) ml = bld<AUX_SC1>(rw, WS_ML + (grp * 16 + h) * 512 + l32 * 16, 0);
    SB();
    if constexpr ((n & 1) == 0) {
      asm volatile("" : "+s"(kvb));
      kvo = (kvb + ((n & 2) << 14) + ((n & 60) << 15)) & (SK * DH * 2 - 1);
    }
    ring[n % WIN] = bld<AUX_NT>(n < NLD ? rk : rv, l32 * 16 + hh * 2048 + (n & 1) * 1024, kvo);
    SB();
    static_for<0, 32>(LAMBDA(jc) {
      constexpr int j = decltype(jc)::value;
      if constexpr (n == (j < 4 ? WM0 + j * WMS : WO0 + 3 * j)) {
        wb[j] = bld<0>(ro, 32 * rc * (DM * 2) + c8 * 16 + (j & 3) * (DM * 2), (unsigned)(((j >> 2) ^ r) * 4 * (DM * 2)));
        SB();
      }
    });
    constexpr int P0 = WO0 + DIST + 1;
    static_assert(P0 + 31 * 3 < 2 * NLD, "all wo rows are paired inside the stream");
    if constexpr (n >= P0 && (n - P0) % 3 == 0 && (n - P0) / 3 < 32 && (((n - P0) / 3) & 1)) {
      constexpr int j = (n - P0) / 3, t = j >> 1;
#pragma unroll
      for (int jj = 0; jj < 4; ++jj) {
        unsigned lo = __builtin_amdgcn_perm(wb[2 * t + 1][jj], wb[2 * t][jj], 0x05040100u);
        unsigned hi = __builtin_amdgcn_perm(wb[2 * t + 1][jj], wb[2 * t][jj], 0x07060302u);
        asm volatile("" : "+v"(lo), "+v"(hi));
        bt[2 * jj][t >> 3][t & 7] = lo;
        bt[2 * jj + 1][t >> 3][t & 7] = hi;
      }
      if constexpr (j == 31) {
#pragma unroll
        for (int jj = 0; jj < 8; ++jj) asm volatile("" : "+v"(bt[jj][0]), "+v"(bt[jj][1]));
      }
      SB();
    }
  };
  auto after = LAMBDA(gc) {
    constexpr int n = issue_after(decltype(gc)::value);
    SB();
    if constexpr (n >= 0) issue(std::integral_constant<int, n>{});
  };

  static_for<0, WIN>(issue);
  if (__builtin_expect(!__all(tagged16(qg, tag)), 0)) {
    for (unsigned spins = 0; spins < SPIN_CAP; ++spins) {
      asm volatile("" ::: "memory");
      qg = bld<AUX_SC1>(rw, WS_Q + (team * 2 + b) * 1024 + l16, 0);
      if (__all(tagged16(qg, tag))) break;
    }
  }
  v4u qv;
  {
    const unsigned q0 = __builtin_amdgcn_perm(qg[1], qg[0], 0x05040100u), q1 = __builtin_amdgcn_perm(qg[3], qg[2], 0x05040100u);
    auto e = __builtin_amdgcn_permlane32_swap(q0, q0, false, false);
    auto o = __builtin_amdgcn_permlane32_swap(q1, q1, false, false);
    qv = v4u{e[0], o[0], e[1], o[1]};
  }
  ST(2);

  float sc[2];
  {
    float xs[16];
    float red[2];
    static_for<0, NLD>(LAMBDA(tc) {
      constexpr int t = decltype(tc)::value;
      const v4u cur = ring[t % WIN];
      xs[t & 15] = dot2(cur[3], qv[3], dot2(cur[2], qv[2], dot2(cur[1], qv[1], dot2(cur[0], qv[0], 0.f))));
      after(tc);
      if constexpr ((t & 15) == 15) {
#define TR_STEP(HALF, CTRL)                                               \
  _Pragma("unroll") for (int u = 0; u < (HALF); ++u) {                    \
    const float lo_ = xs[u], hi_ = xs[u + (HALF)];                        \
    const float t1_ = lo_ + dppf<CTRL>(lo_), t2_ = hi_ + dppf<CTRL>(hi_); \
    xs[u] = (lane & (HALF)) ? t2_ : t1_;                                  \
  }
        TR_STEP(8, XOR8) TR_STEP(4, XOR7) TR_STEP(2, XOR2) TR_STEP(1, XOR1)
#undef TR_STEP
        asm volatile("" : "+v"(xs[0]));
        red[(t >> 4) & 1] = xs[0];
        if constexpr ((t & 31) == 31) sc[t >> 5] = swap16_add(red[0], red[1]);
        SB();
      }
    });
  }
  ST(3);
  constexpr float C2 = 0.0625f * 1.4426950408889634f;
  const float mw = swap32_max(half_max(fmaxf(sc[0], sc[1]))) * C2;
  {
    const float p0 = __builtin_amdgcn_exp2f(fmaf(sc[0], C2, -mw));
    const float p1 = __builtin_amdgcn_exp2f(fmaf(sc[1], C2, -mw));
    lds[L_P + w * 128 + hh * 64 + l32] = p0;
    lds[L_P + w * 128 + hh * 64 + 32 + l32] = p1;
    float lw = half_sum(p0 + p1);
    lw = swap32_add(lw, lw);
    unsigned arrived = 0;
    if (lane == 0) {
      *(f32x2*)(lds + L_ML + w * 2) = f32x2{mw, lw};
      asm volatile("" ::: "memory");
      arrived = __hip_atomic_fetch_add((unsigned*)lds + L_CNT + 1, 1u, __ATOMIC_RELAXED, __HIP_MEMORY_SCOPE_WORKGROUP);
    }
    asm volatile("" ::: "memory");
    if (__builtin_amdgcn_readfirstlane(arrived) == 7) {
      const v2u e = *(const v2u*)(lds + L_ML + c8 * 2);
      __builtin_amdgcn_raw_buffer_store_b128(v4u{__builtin_amdgcn_perm(tag, e[0], 0x05040100u), __builtin_amdgcn_perm(tag, e[0], 0x07060302u),
                                                 __builtin_amdgcn_perm(tag, e[1], 0x05040100u), __builtin_amdgcn_perm(tag, e[1], 0x07060302u)}, rw,
                                             (lane < 8 ? WS_ML : OOB) + (grp * 16 + h) * 512 + (s * 8 + c8) * 16, 0, AUX_SC1);
    }
  }
  unsigned yst = (w < 7 ? WS_Y : OOB) + ((grp * 14 + w * 2 + hh) * 64 + me) * 128 + l32 * 4;
  unsigned ytag = (tag >> ((lane & 3) * 8)) & 255u;
  unsigned accrd = L_ACC * 4 + rc * 1024 + w * 128 + c8 * 16;
  const int kk = c8 ^ r;
  unsigned owr = L_O * 4 + (((kk >> 2) * 4 + ((kk >> 1) & 1) * 2 + (w >> 2)) * 16 + hh * 8 + (w & 1) * 4 + ((w >> 1) & 1) * 2 + (kk & 1)) * 4;
  asm volatile("s_waitcnt lgkmcnt(0)" : "+v"(yst), "+v"(ytag), "+v"(accrd), "+v"(owr)::"memory");
  SB();

  f32x2 acc[4];
#pragma unroll
  for (int j = 0; j < 4; ++j) acc[j] = f32x2{0.f, 0.f};
  float fw;
  bool mlok;
  auto ml_eval = [&]() __attribute__((always_inline)) {
    mlok = __all(tagged16(ml, tag));
    const float m = __uint_as_float(__builtin_amdgcn_perm(ml[1], ml[0], 0x05040100u));
    const float M = half_max(m);
    const float L = half_sum(__builtin_amdgcn_exp2f(m - M) * __uint_as_float(__builtin_amdgcn_perm(ml[3], ml[2], 0x05040100u)));
    fw = __builtin_amdgcn_exp2f(mw - M) / L;
  };
  {
    const f32x4* pl = (const f32x4*)(lds + L_P + w * 128 + hh * 64);
    f32x4 pc = pl[0];
    static_for<0, NLD>(LAMBDA(tc) {
      constexpr int t = decltype(tc)::value;
      const v4u cur = ring[(t + NLD) % WIN];
      const float pv_ = pc[t & 3];
      if constexpr ((t & 3) == 3 && t + 1 < NLD) pc = pl[(t + 1) >> 2];
#pragma unroll
      for (int j = 0; j < 4; ++j) {
        const f32x2 vv = {__uint_as_float(cur[j] << 16), __uint_as_float(cur[j] & 0xffff0000u)};
        acc[j] = __builtin_elementwise_fma(vv, f32x2{pv_, pv_}, acc[j]);
      }
      asm volatile("" : "+v"(acc[0]), "+v"(acc[1]), "+v"(acc[2]), "+v"(acc[3]));
      after(std::integral_constant<int, t + NLD>{});
      if constexpr (t + NLD == G_ML) {
        ml_eval();
        asm volatile("" : "+v"(fw));
        SB();
      }
    });
  }
  if (__builtin_expect(!mlok, 0)) {
    for (unsigned spins = 0; !mlok && spins < SPIN_CAP; ++spins) {
      __builtin_amdgcn_s_sleep(8);
      asm volatile("" ::: "memory");
      ml = bld<AUX_SC1>(rw, WS_ML + (grp * 16 + h) * 512 + l32 * 16, 0);
      ml_eval();
    }
  }
  ST(4);

  {
    f32x4 rr;
#pragma unroll
    for (int j = 0; j < 4; ++j) rr[j] = swap32_add(acc[j >> 1][j & 1], acc[2 + (j >> 1)][j & 1]) * fw;
    *(f32x4*)(lds + L_ACC + w * 256 + l32 * 8 + hh * 4) = rr;
  }
  LDS_BARRIER();
  const f32x4 a = *(const f32x4*)((const char*)lds + accrd);
  SB();
  int sidx = (lane & 2) ? (int)0xEEEEEEEEu : 0x44444444;
  unsigned ord = L_O * 4 + (lane >> 4) * 64 + (lane & 3) * 16;
  f32x4 dd[8];
#pragma unroll
  for (int j = 0; j < 8; ++j) {
    dd[j] = f32x4{0.f, 0.f, 0.f, 0.f};
    asm volatile("" : "+v"(dd[j]));
  }
  asm volatile("" : "+v"(sidx), "+v"(ord));
  SB();
  {
    float e0 = swap32_add(a[0], a[2]), e1 = swap32_add(a[1], a[3]);
    e0 += dppf<XOR8>(e0);
    e1 += dppf<XOR8>(e1);
    e0 = swap16_add(e0, e0);
    e1 = swap16_add(e1, e1);
    if ((lane & 24) == 0) *(unsigned*)((char*)lds + owr) = pack_bf16(e0, e1);
  }
  LDS_BARRIER();

  {
    const v4u oa0 = *(const v4u*)((const char*)lds + ord), oa1 = *(const v4u*)((const char*)lds + ord + 256);
#pragma unroll
    for (int j = 0; j < 8; ++j)
      dd[j] = __builtin_amdgcn_smfmac_f32_16x16x64_bf16(__builtin_bit_cast(bf16x8, oa0), __builtin_bit_cast(bf16x16, bt[j][0]), dd[j], sidx, 0, 0);
#pragma unroll
    for (int j = 0; j < 8; ++j)
      dd[j] = __builtin_amdgcn_smfmac_f32_16x16x64_bf16(__builtin_bit_cast(bf16x8, oa1), __builtin_bit_cast(bf16x16, bt[j][1]), dd[j], sidx, 0, 0);
    SB();
    float y[8];
#pragma unroll
    for (int j = 0; j < 8; ++j) y[j] = sel(lane & 8, dd[j][1] + dd[j][3], dd[j][0] + dd[j][2]);
    float u[2];
#pragma unroll
    for (int j = 0; j < 2; ++j) u[j] = sel(hh, sel(lane & 16, y[6 + j], y[4 + j]), sel(lane & 16, y[2 + j], y[j]));
    const float t0 = u[0] + dppf<XOR8>(u[0]), t1 = u[1] + dppf<XOR8>(u[1]);
    const float yv = (lane & 8) ? t1 : t0;
    __builtin_amdgcn_raw_buffer_store_b32((__float_as_uint(yv) & ~255u) | ytag, rw, yst, 0, AUX_SC1);
    ST(5);
  }
  if (w < 7 || me >= 14) { ST_FLUSH(); return; }

  {
    const unsigned yo = WS_Y + (grp * 14 + me) * 8192 + l16;
    const v4u tg = {tag & 255u, (tag >> 8) & 255u, (tag >> 16) & 255u, tag >> 24};
    const int ls = 32 * (me & 1) + 4 * c8 + 2 * hh + ((lane >> 4) & 1);
    GAS unsigned short* optr = (GAS unsigned short*)prm.out + b * (2 * DM) + blk * DM + 64 * (me >> 1) + 8 * (ls & 7) + (ls >> 3);
    __builtin_amdgcn_s_sleep(16);
    f32x4 sum;
    for (unsigned spins = 0;; ++spins) {
      v4u g[8];
#pragma unroll
      for (int t = 0; t < 8; ++t) g[t] = bld<AUX_SC1>(rw, yo, t * 1024);
      unsigned bad = 0;
      sum = f32x4{0.f, 0.f, 0.f, 0.f};
#pragma unroll
      for (int t = 0; t < 8; ++t) {
        const v4u x = g[t] ^ tg;
        bad |= (x[0] | x[1]) | (x[2] | x[3]);
        sum += __builtin_bit_cast(f32x4, x);
      }
      if (__all((bad & 255u) == 0) || spins > SPIN_CAP) break;
      asm volatile("" ::: "memory");
    }
    ST(6);
    float rr = swap16_add(swap32_add(sum[0], sum[2]), swap32_add(sum[1], sum[3]));
    rr += dppf<XOR8>(rr);
    if ((lane & 8) == 0) __hip_atomic_store(optr, (unsigned short)(pack_bf16(rr, rr) & 0xffffu), RLX_AGENT);
    if (me == 0 && b == 0 && lane == 0) __hip_atomic_store((GAS unsigned*)((GAS char*)prm.ws + WS_EPOCH + blk * 256), tag, RLX_AGENT);
    {
      const int el = (int)((unsigned)__builtin_amdgcn_s_memrealtime() - t_amp);
      const int tgt = 10000 + 10 * (int)(ZM == 0 ? t_k - t_amp : t_start - t_k);
      while ((int)((unsigned)__builtin_amdgcn_s_memrealtime() - t_amp) < tgt) __builtin_amdgcn_s_sleep(1);
    }
    ST(7);
    ST_FLUSH();
  }
}

extern "C" void kernel_launch(void* const* d_in, const int* in_sizes, int n_in, void* d_out, int out_size, void* d_ws,
                              size_t ws_size, hipStream_t stream) {
  Params p;
  p.x[0] = d_in[0];
  p.k[0] = d_in[1];
  p.v[0] = d_in[2];
  p.wq[0] = d_in[3];
  p.wo[0] = d_in[4];
  p.x[1] = d_in[5];
  p.k[1] = d_in[6];
  p.v[1] = d_in[7];
  p.wq[1] = d_in[8];
  p.wo[1] = d_in[9];
  p.out = d_out;
  p.ws = d_ws;
  mha2_decode_kernel<<<dim3(256), dim3(NTHR), 0, stream>>>(p);
}
```

```cpp
#include <hip/hip_runtime.h>
#include <hip/hip_bf16.h>
#include <stdint.h>
#include <type_traits>

#define GAS __attribute__((address_space(1)))
#define CAS __attribute__((address_space(4)))
typedef unsigned v2u __attribute__((ext_vector_type(2)));
typedef unsigned v4u __attribute__((ext_vector_type(4)));
typedef unsigned v8u __attribute__((ext_vector_type(8)));
typedef float f32x2 __attribute__((ext_vector_type(2)));
typedef float f32x4 __attribute__((ext_vector_type(4)));
typedef __bf16 bf16x2 __attribute__((ext_vector_type(2)));
typedef __bf16 bf16x8 __attribute__((ext_vector_type(8)));
typedef __bf16 bf16x16 __attribute__((ext_vector_type(16)));
#define RLX_AGENT __ATOMIC_RELAXED, __HIP_MEMORY_SCOPE_AGENT

constexpr int WIN = 16;
constexpr int DIST = 6;
constexpr int FREE_N = 4;
constexpr int N_WQ = 2;
constexpr int N_QCALC = 6;
constexpr int N_QPOLL = 13;
constexpr int WM0 = 6, WMS = 2;
constexpr int WO0 = 12;
constexpr int N_ML = 96;
constexpr int G_ML = 123;
constexpr int PACE_T0 = -10;
constexpr int PACE_DUR = 3580;
constexpr int PACE_GAIN = 128;

constexpr int H = 16, SK = 4096, DH = 256, DM = 448;
constexpr int NTHR = 512;
constexpr int NLD = 64;
constexpr unsigned WS_EPOCH = 0;
constexpr unsigned WS_Q = 4096;
constexpr unsigned WS_ML = WS_Q + 65536;
constexpr unsigned WS_Y = WS_ML + 32768;
constexpr unsigned WS_END = WS_Y + 4 * 14 * 8192;
constexpr unsigned SPIN_CAP = 1u << 20;
constexpr unsigned OOB = 0x7fffff00u;
constexpr int AUX_NT = 2, AUX_SC1 = 16;

struct Params {
  const void* k[2];
  const void* wq[2];
  const void* x[2];
  void* ws;
  void* out;
  const void* v[2];
  const void* wo[2];
};

__device__ __forceinline__ float dot2(unsigned a, unsigned b, float c) {
  return __builtin_amdgcn_fdot2_f32_bf16(__builtin_bit_cast(bf16x2, a), __builtin_bit_cast(bf16x2, b), c, false);
}
__device__ __forceinline__ unsigned pack_bf16(float lo, float hi) {
  bf16x2 r;
  r.x = (__bf16)lo;
  r.y = (__bf16)hi;
  return __builtin_bit_cast(unsigned, r);
}
template <int CTRL> __device__ __forceinline__ float dppf(float x) {
  return __builtin_bit_cast(float, __builtin_amdgcn_mov_dpp(__builtin_bit_cast(int, x), CTRL, 0xf, 0xf, true));
}
constexpr int XOR1 = 0xB1, XOR2 = 0x4E, XOR7 = 0x141, XOR8 = 0x128, ROR4 = 0x124;
__device__ __forceinline__ float swap16_add(float a, float b) {
  auto r = __builtin_amdgcn_permlane16_swap(__float_as_uint(a), __float_as_uint(b), false, false);
  return __uint_as_float(r[0]) + __uint_as_float(r[1]);
}
__device__ __forceinline__ float swap32_add(float a, float b) {
  auto r = __builtin_amdgcn_permlane32_swap(__float_as_uint(a), __float_as_uint(b), false, false);
  return __uint_as_float(r[0]) + __uint_as_float(r[1]);
}
__device__ __forceinline__ float swap32_max(float a) {
  auto r = __builtin_amdgcn_permlane32_swap(__float_as_uint(a), __float_as_uint(a), false, false);
  return fmaxf(__uint_as_float(r[0]), __uint_as_float(r[1]));
}
__device__ __forceinline__ float half_max(float v) {
  v = fmaxf(v, dppf<XOR1>(v));
  v = fmaxf(v, dppf<XOR2>(v));
  v = fmaxf(v, dppf<XOR7>(v));
  v = fmaxf(v, dppf<XOR8>(v));
  auto r = __builtin_amdgcn_permlane16_swap(__float_as_uint(v), __float_as_uint(v), false, false);
  return fmaxf(__uint_as_float(r[0]), __uint_as_float(r[1]));
}
__device__ __forceinline__ float half_sum(float v) {
  v += dppf<XOR1>(v);
  v += dppf<XOR2>(v);
  v += dppf<XOR7>(v);
  v += dppf<XOR8>(v);
  return swap16_add(v, v);
}
__device__ __forceinline__ float sel(bool c, float a, float b) {
  asm volatile("" : "+v"(a), "+v"(b));
  return c ? a : b;
}
__device__ __forceinline__ bool tagged16(v4u g, unsigned tag) {
  return __builtin_amdgcn_perm(g[1], g[0], 0x07060302u) == tag && __builtin_amdgcn_perm(g[3], g[2], 0x07060302u) == tag;
}
template <int I, int N, class F> __device__ __forceinline__ void static_for(F&& f) {
  if constexpr (I < N) {
    f(std::integral_constant<int, I>{});
    static_for<I + 1, N>(f);
  }
}
#define LAMBDA(v) [&](auto v) __attribute__((always_inline))
typedef __amdgpu_buffer_rsrc_t rsrc_t;
__device__ __forceinline__ rsrc_t mk_rsrc(const void* base, unsigned bytes) {
  return __builtin_amdgcn_make_buffer_rsrc((void*)base, 0, bytes, 0x00020000);
}
template <int AUX> __device__ __forceinline__ v4u bld(rsrc_t rs, unsigned voff, unsigned soff) {
  return __builtin_amdgcn_raw_buffer_load_b128(rs, voff, soff, AUX);
}
#define SB() __builtin_amdgcn_sched_barrier(0)
#define LDS_BARRIER()                                  \
  do {                                                 \
    asm volatile("s_waitcnt lgkmcnt(0)" ::: "memory"); \
    __builtin_amdgcn_s_barrier();                      \
    asm volatile("" ::: "memory");                     \
    SB();                                              \
  } while (0)

constexpr int pace_thr(int n, int depth) {
  const int dith16 = 2 * (((n & 1) << 2) | (n & 2) | ((n & 4) >> 2)) + 1;
  const int lag = depth == 2   ? -50
                  : depth <= 4 ? PACE_GAIN * (16 * (depth - 2) - dith16) / 16
                               : 2 * PACE_GAIN + (PACE_GAIN + 300 * n / 128) * (16 - dith16) / 16;
  return PACE_T0 + n * PACE_DUR / 128 + lag;
}
template <int N> __device__ __forceinline__ void throttle(unsigned t_start) {
  const int el = (int)((unsigned)__builtin_amdgcn_s_memrealtime() - t_start);
  asm volatile(
      "s_waitcnt vmcnt(5)\n\ts_cmpk_ge_i32 %0, %1\n\ts_cbranch_scc1 1f\n\ts_waitcnt vmcnt(3)\n\t"
      "s_cmpk_ge_i32 %0, %2\n\ts_cbranch_scc1 1f\n\ts_waitcnt vmcnt(2)\n\t"
      "s_cmpk_ge_i32 %0, %3\n\ts_cbranch_scc1 1f\n\ts_waitcnt vmcnt(1)\n\t"
      "s_cmpk_ge_i32 %0, %4\n\ts_cbranch_scc1 1f\n\ts_waitcnt vmcnt(0)\n1:" ::"s"(el),
      "i"(pace_thr(N, 5)), "i"(pace_thr(N, 4)), "i"(pace_thr(N, 3)), "i"(pace_thr(N, 2))
      : "scc");
}
constexpr int issue_after(int g) {
  const int n = g < 2 * (WIN - DIST) ? ((g & 1) ? WIN + g / 2 : -1) : g + DIST;
  return n < 2 * NLD ? n : -1;
}

constexpr int L_QP = 0;
constexpr int L_P = L_QP + 8 * 64;
constexpr int L_ACC = L_P + 8 * 128;
constexpr int L_O = L_ACC + 8 * 256;
constexpr int L_ML = L_O + 128;
constexpr int L_CNT = L_ML + 16;
constexpr int L_END = L_CNT + 4;

__global__ void __launch_bounds__(NTHR, 2) mha2_decode_kernel(Params prm) {
  __shared__ __attribute__((aligned(16))) float lds[L_END];
  SB();
  const int tid = threadIdx.x, lane = tid & 63;
  const int w = __builtin_amdgcn_readfirstlane(tid >> 6);
  const int bid = blockIdx.x;
  const int team = (bid & 7) * 4 + (bid >> 6);
  const int blk = team >> 4, h = team & 15;
  const int r = (bid >> 3) & 7, b = r >> 2, s = r & 3;
  const int hh = lane >> 5, l32 = lane & 31, rc = lane >> 3, c8 = lane & 7;
  const int grp = blk * 2 + b, me = s * 16 + h;
  unsigned kvoff = (unsigned)(b * H + h) * (SK * DH * 2);
  unsigned kvb = (unsigned)(grp * 16 + h) * 32768u + ((w & 1) << 9) + ((w & 6) << 11) + ((s & 1) << 14) + ((s & 2) << 15);
  unsigned kvl = l32 * 16 + hh * 2048;
  asm volatile("" : "+s"(kvoff), "+s"(kvb), "+v"(kvl));
  SB();
  const char* const pk = (const char*)(blk ? prm.k[1] : prm.k[0]);
  const char* const pv = (const char*)(blk ? prm.v[1] : prm.v[0]);
  const char* const pwq = (const char*)(blk ? prm.wq[1] : prm.wq[0]);
  const char* const pwo = (const char*)(blk ? prm.wo[1] : prm.wo[0]);
  const char* const px = (const char*)(blk ? prm.x[1] : prm.x[0]);
  const rsrc_t rk = mk_rsrc(pk + kvoff, SK * DH * 2);
  const rsrc_t rv = mk_rsrc(pv + kvoff, SK * DH * 2);
  unsigned kvo = 0;
  const unsigned l16 = lane * 16;
  const rsrc_t rw = mk_rsrc(prm.ws, WS_END);
  const rsrc_t rq = mk_rsrc(pwq + (size_t)(w * 56) * (H * DH * 2) + h * (DH * 2) + r * 64, 55 * (H * DH * 2) + 64);
  const rsrc_t rx = mk_rsrc(px + w * 112, DM * 2 + 112);
  const rsrc_t ro = mk_rsrc(pwo + (size_t)h * DH * (DM * 2) + w * 128, w < 7 ? 255 * (DM * 2) + 128 : 0);

  unsigned tag, t_start;
  v4u ring[WIN];
  v4u wqv[4], qg, ml;
  v2u x0, x1;
  v4u wb[32];
  v8u bt[8][2];

  auto q_share = [&]() __attribute__((always_inline)) {
    float a0[8], a1[8];
#pragma unroll
    for (int j = 0; j < 4; ++j) {
      const unsigned p0 = __builtin_amdgcn_perm(wqv[1][j], wqv[0][j], 0x05040100u), p1 = __builtin_amdgcn_perm(wqv[1][j], wqv[0][j], 0x07060302u);
      const unsigned p2 = __builtin_amdgcn_perm(wqv[3][j], wqv[2][j], 0x05040100u), p3 = __builtin_amdgcn_perm(wqv[3][j], wqv[2][j], 0x07060302u);
      a0[2 * j] = dot2(p2, x0[1], dot2(p0, x0[0], 0.f));
      a0[2 * j + 1] = dot2(p3, x0[1], dot2(p1, x0[0], 0.f));
      a1[2 * j] = dot2(p2, x1[1], dot2(p0, x1[0], 0.f));
      a1[2 * j + 1] = dot2(p3, x1[1], dot2(p1, x1[0], 0.f));
    }
    float z[8];
#pragma unroll
    for (int j = 0; j < 8; ++j) z[j] = swap32_add(a0[j], a1[j]);
    f32x4 u;
#pragma unroll
    for (int j = 0; j < 4; ++j) {
      float t = swap16_add(z[j], z[j + 4]);
      t += dppf<ROR4>(t);
      t += dppf<XOR8>(t);
      u[j] = t;
    }
    if ((lane & 12) == 0) *(f32x4*)(lds + L_QP + w * 64 + hh * 32 + (lane & 3) * 8 + ((lane >> 4) & 1) * 4) = u;
    unsigned arrived = 0;
    asm volatile("s_waitcnt lgkmcnt(0)" : "+s"(tag)::"memory");
    if (lane == 0) arrived = __hip_atomic_fetch_add((unsigned*)lds + L_CNT, 1u, __ATOMIC_RELAXED, __HIP_MEMORY_SCOPE_WORKGROUP);
    asm volatile("" ::: "memory");
    if (__builtin_amdgcn_readfirstlane(arrived) == 7) {
      float q = 0.f;
#pragma unroll
      for (int i = 0; i < 8; ++i) q += lds[L_QP + i * 64 + lane];
      __builtin_amdgcn_raw_buffer_store_b32((pack_bf16(q, q) & 0xffffu) | (((lane & 1) ? tag : tag << 16) & 0xffff0000u), rw,
                                            WS_Q + (team * 2 + hh) * 1024 + ((lane >> 2) & 1) * 512 + (r * 4 + (l32 >> 3)) * 16 + (lane & 3) * 4, 0, AUX_SC1);
    }
    SB();
  };

  auto issue = LAMBDA(nc) {
    constexpr int n = decltype(nc)::value;
    if constexpr (n == N_WQ) {
      t_start = (unsigned)__builtin_amdgcn_s_memrealtime();
      if (tid < 2) ((unsigned*)lds)[L_CNT + tid] = 0u;
      LDS_BARRIER();
      tag = *(const CAS unsigned*)((const CAS char*)prm.ws + WS_EPOCH + blk * 256) + 0x01010101u;
      const unsigned xoff = lane < 56 ? (lane >> 2) * 8 : OOB;
      x0 = __builtin_amdgcn_raw_buffer_load_b64(rx, xoff, 0, 0);
      x1 = __builtin_amdgcn_raw_buffer_load_b64(rx, xoff, DM * 2, 0);
#pragma unroll
      for (int i = 0; i < 4; ++i) wqv[i] = bld<0>(rq, (lane >> 2) * 4 * (H * DH * 2) + (lane & 3) * 16, i * (H * DH * 2));
    }
    if constexpr (n == N_QCALC) q_share();
    if constexpr (n >= FREE_N) throttle<n>(t_start);
    if constexpr (n == N_QPOLL) qg = bld<AUX_SC1>(rw, WS_Q + (team * 2 + b) * 1024 + l16, 0);
    if constexpr (n == N_ML) ml = bld<AUX_SC1>(rw, WS_ML + (grp * 16 + h) * 512 + l32 * 16, 0);
    SB();
    if constexpr ((n & 1) == 0) {
      asm volatile("" : "+s"(kvb));
      kvo = (kvb + ((n & 2) << 14) + ((n & 60) << 15)) & (SK * DH * 2 - 1);
    }
    ring[n % WIN] = bld<AUX_NT>(n < NLD ? rk : rv, kvl + (n & 1) * 1024, kvo);
    SB();
    static_for<0, 32>(LAMBDA(jc) {
      constexpr int j = decltype(jc)::value;
      if constexpr (n == (j < 4 ? WM0 + j * WMS : WO0 + 3 * j)) {
        wb[j] = bld<0>(ro, 32 * rc * (DM * 2) + c8 * 16 + (j & 3) * (DM * 2), (unsigned)(((j >> 2) ^ r) * 4 * (DM * 2)));
        SB();
      }
    });
    constexpr int P0 = WO0 + DIST + 1;
    static_assert(P0 + 31 * 3 < 2 * NLD, "all wo rows are paired inside the stream");
    if constexpr (n >= P0 && (n - P0) % 3 == 0 && (n - P0) / 3 < 32 && (((n - P0) / 3) & 1)) {
      constexpr int j = (n - P0) / 3, t = j >> 1;
#pragma unroll
      for (int jj = 0; jj < 4; ++jj) {
        unsigned lo = __builtin_amdgcn_perm(wb[2 * t + 1][jj], wb[2 * t][jj], 0x05040100u);
        unsigned hi = __builtin_amdgcn_perm(wb[2 * t + 1][jj], wb[2 * t][jj], 0x07060302u);
        asm volatile("" : "+v"(lo), "+v"(hi));
        bt[2 * jj][t >> 3][t & 7] = lo;
        bt[2 * jj + 1][t >> 3][t & 7] = hi;
      }
      if constexpr (j == 31) {
#pragma unroll
        for (int jj = 0; jj < 8; ++jj) asm volatile("" : "+v"(bt[jj][0]), "+v"(bt[jj][1]));
      }
      SB();
    }
  };
  auto after = LAMBDA(gc) {
    constexpr int n = issue_after(decltype(gc)::value);
    SB();
    if constexpr (n >= 0) issue(std::integral_constant<int, n>{});
  };

  static_for<0, WIN>(issue);
  if (__builtin_expect(!__all(tagged16(qg, tag)), 0)) {
    for (unsigned spins = 0; spins < SPIN_CAP; ++spins) {
      asm volatile("" ::: "memory");
      qg = bld<AUX_SC1>(rw, WS_Q + (team * 2 + b) * 1024 + l16, 0);
      if (__all(tagged16(qg, tag))) break;
    }
  }
  v4u qv;
  {
    const unsigned q0 = __builtin_amdgcn_perm(qg[1], qg[0], 0x05040100u), q1 = __builtin_amdgcn_perm(qg[3], qg[2], 0x05040100u);
    auto e = __builtin_amdgcn_permlane32_swap(q0, q0, false, false);
    auto o = __builtin_amdgcn_permlane32_swap(q1, q1, false, false);
    qv = v4u{e[0], o[0], e[1], o[1]};
  }

  float sc[2];
  {
    float xs[16];
    float red[2];
    static_for<0, NLD>(LAMBDA(tc) {
      constexpr int t = decltype(tc)::value;
      const v4u cur = ring[t % WIN];
      xs[t & 15] = dot2(cur[3], qv[3], dot2(cur[2], qv[2], dot2(cur[1], qv[1], dot2(cur[0], qv[0], 0.f))));
      asm volatile("" : "+v"(xs[t & 15]));
      after(tc);
      if constexpr ((t & 15) == 15) {
#define TR_STEP(HALF, CTRL)                                               \
  _Pragma("unroll") for (int u = 0; u < (HALF); ++u) {                    \
    const float lo_ = xs[u], hi_ = xs[u + (HALF)];                        \
    const float t1_ = lo_ + dppf<CTRL>(lo_), t2_ = hi_ + dppf<CTRL>(hi_); \
    xs[u] = (lane & (HALF)) ? t2_ : t1_;                                  \
  }
        TR_STEP(8, XOR8) TR_STEP(4, XOR7) TR_STEP(2, XOR2) TR_STEP(1, XOR1)
#undef TR_STEP
        asm volatile("" : "+v"(xs[0]));
        red[(t >> 4) & 1] = xs[0];
        if constexpr ((t & 31) == 31) sc[t >> 5] = swap16_add(red[0], red[1]);
        SB();
      }
    });
  }
  constexpr float C2 = 0.0625f * 1.4426950408889634f;
  const float mw = swap32_max(half_max(fmaxf(sc[0], sc[1]))) * C2;
  {
    const float p0 = __builtin_amdgcn_exp2f(fmaf(sc[0], C2, -mw));
    const float p1 = __builtin_amdgcn_exp2f(fmaf(sc[1], C2, -mw));
    lds[L_P + w * 128 + hh * 64 + l32] = p0;
    lds[L_P + w * 128 + hh * 64 + 32 + l32] = p1;
    float lw = half_sum(p0 + p1);
    lw = swap32_add(lw, lw);
    unsigned arrived = 0;
    if (lane == 0) {
      *(f32x2*)(lds + L_ML + w * 2) = f32x2{mw, lw};
      asm volatile("" ::: "memory");
      arrived = __hip_atomic_fetch_add((unsigned*)lds + L_CNT + 1, 1u, __ATOMIC_RELAXED, __HIP_MEMORY_SCOPE_WORKGROUP);
    }
    asm volatile("" ::: "memory");
    if (__builtin_amdgcn_readfirstlane(arrived) == 7) {
      const v2u e = *(const v2u*)(lds + L_ML + c8 * 2);
      __builtin_amdgcn_raw_buffer_store_b128(v4u{__builtin_amdgcn_perm(tag, e[0], 0x05040100u), __builtin_amdgcn_perm(tag, e[0], 0x07060302u),
                                                 __builtin_amdgcn_perm(tag, e[1], 0x05040100u), __builtin_amdgcn_perm(tag, e[1], 0x07060302u)}, rw,
                                             (lane < 8 ? WS_ML : OOB) + (grp * 16 + h) * 512 + (s * 8 + c8) * 16, 0, AUX_SC1);
    }
  }
  unsigned yst = (w < 7 ? WS_Y : OOB) + ((grp * 14 + w * 2 + hh) * 64 + me) * 128 + l32 * 4;
  unsigned ytag = (tag >> ((lane & 3) * 8)) & 255u;
  unsigned accrd = L_ACC * 4 + rc * 1024 + w * 128 + c8 * 16;
  const int kk = c8 ^ r;
  unsigned owr = L_O * 4 + (((kk >> 2) * 4 + ((kk >> 1) & 1) * 2 + (w >> 2)) * 16 + hh * 8 + (w & 1) * 4 + ((w >> 1) & 1) * 2 + (kk & 1)) * 4;
  asm volatile("s_waitcnt lgkmcnt(0)" : "+v"(yst), "+v"(ytag), "+v"(accrd), "+v"(owr)::"memory");
  SB();

  f32x2 acc[4];
#pragma unroll
  for (int j = 0; j < 4; ++j) acc[j] = f32x2{0.f, 0.f};
  float fw;
  bool mlok;
  auto ml_eval = [&]() __attribute__((always_inline)) {
    mlok = __all(tagged16(ml, tag));
    const float m = __uint_as_float(__builtin_amdgcn_perm(ml[1], ml[0], 0x05040100u));
    const float M = half_max(m);
    const float L = half_sum(__builtin_amdgcn_exp2f(m - M) * __uint_as_float(__builtin_amdgcn_perm(ml[3], ml[2], 0x05040100u)));
    fw = __builtin_amdgcn_exp2f(mw - M) / L;
  };
  {
    const f32x4* pl = (const f32x4*)(lds + L_P + w * 128 + hh * 64);
    f32x4 pc = pl[0];
    static_for<0, NLD>(LAMBDA(tc) {
      constexpr int t = decltype(tc)::value;
      const v4u cur = ring[(t + NLD) % WIN];
      const float pv_ = pc[t & 3];
      if constexpr ((t & 3) == 3 && t + 1 < NLD) pc = pl[(t + 1) >> 2];
#pragma unroll
      for (int j = 0; j < 4; ++j) {
        const f32x2 vv = {__uint_as_float(cur[j] << 16), __uint_as_float(cur[j] & 0xffff0000u)};
        acc[j] = __builtin_elementwise_fma(vv, f32x2{pv_, pv_}, acc[j]);
      }
      asm volatile("" : "+v"(acc[0]), "+v"(acc[1]), "+v"(acc[2]), "+v"(acc[3]));
      after(std::integral_constant<int, t + NLD>{});
      if constexpr (t + NLD == G_ML) {
        ml_eval();
        asm volatile("" : "+v"(fw));
        SB();
      }
    });
  }
  if (__builtin_expect(!mlok, 0)) {
    for (unsigned spins = 0; !mlok && spins < SPIN_CAP; ++spins) {
      __builtin_amdgcn_s_sleep(8);
      asm volatile("" ::: "memory");
      ml = bld<AUX_SC1>(rw, WS_ML + (grp * 16 + h) * 512 + l32 * 16, 0);
      ml_eval();
    }
  }

  {
    f32x4 rr;
#pragma unroll
    for (int j = 0; j < 4; ++j) rr[j] = swap32_add(acc[j >> 1][j & 1], acc[2 + (j >> 1)][j & 1]) * fw;
    *(f32x4*)(lds + L_ACC + w * 256 + l32 * 8 + hh * 4) = rr;
  }
  LDS_BARRIER();
  const f32x4 a = *(const f32x4*)((const char*)lds + accrd);
  SB();
  int sidx = (lane & 2) ? (int)0xEEEEEEEEu : 0x44444444;
  unsigned ord = L_O * 4 + (lane >> 4) * 64 + (lane & 3) * 16;
  f32x4 dd[8];
#pragma unroll
  for (int j = 0; j < 8; ++j) {
    dd[j] = f32x4{0.f, 0.f, 0.f, 0.f};
    asm volatile("" : "+v"(dd[j]));
  }
  asm volatile("" : "+v"(sidx), "+v"(ord));
  SB();
  {
    float e0 = swap32_add(a[0], a[2]), e1 = swap32_add(a[1], a[3]);
    e0 += dppf<XOR8>(e0);
    e1 += dppf<XOR8>(e1);
    e0 = swap16_add(e0, e0);
    e1 = swap16_add(e1, e1);
    if ((lane & 24) == 0) *(unsigned*)((char*)lds + owr) = pack_bf16(e0, e1);
  }
  LDS_BARRIER();

  {
    const v4u oa0 = *(const v4u*)((const char*)lds + ord), oa1 = *(const v4u*)((const char*)lds + ord + 256);
#pragma unroll
    for (int j = 0; j < 8; ++j)
      dd[j] = __builtin_amdgcn_smfmac_f32_16x16x64_bf16(__builtin_bit_cast(bf16x8, oa0), __builtin_bit_cast(bf16x16, bt[j][0]), dd[j], sidx, 0, 0);
#pragma unroll
    for (int j = 0; j < 8; ++j)
      dd[j] = __builtin_amdgcn_smfmac_f32_16x16x64_bf16(__builtin_bit_cast(bf16x8, oa1), __builtin_bit_cast(bf16x16, bt[j][1]), dd[j], sidx, 0, 0);
    SB();
    float y[8];
#pragma unroll
    for (int j = 0; j < 8; ++j) y[j] = sel(lane & 8, dd[j][1] + dd[j][3], dd[j][0] + dd[j][2]);
    float u[2];
#pragma unroll
    for (int j = 0; j < 2; ++j) u[j] = sel(hh, sel(lane & 16, y[6 + j], y[4 + j]), sel(lane & 16, y[2 + j], y[j]));
    const float t0 = u[0] + dppf<XOR8>(u[0]), t1 = u[1] + dppf<XOR8>(u[1]);
    const float yv = (lane & 8) ? t1 : t0;
    __builtin_amdgcn_raw_buffer_store_b32((__float_as_uint(yv) & ~255u) | ytag, rw, yst, 0, AUX_SC1);
  }
  if (w < 7 || me >= 14) return;

  {
    const unsigned yo = WS_Y + (grp * 14 + me) * 8192 + l16;
    const v4u tg = {tag & 255u, (tag >> 8) & 255u, (tag >> 16) & 255u, tag >> 24};
    const int ls = 32 * (me & 1) + 4 * c8 + 2 * hh + ((lane >> 4) & 1);
    GAS unsigned short* optr = (GAS unsigned short*)prm.out + b * (2 * DM) + blk * DM + 64 * (me >> 1) + 8 * (ls & 7) + (ls >> 3);
    __builtin_amdgcn_s_sleep(16);
    f32x4 sum;
    for (unsigned spins = 0;; ++spins) {
      v4u g[8];
#pragma unroll
      for (int t = 0; t < 8; ++t) g[t] = bld<AUX_SC1>(rw, yo, t * 1024);
      unsigned bad = 0;
      sum = f32x4{0.f, 0.f, 0.f, 0.f};
#pragma unroll
      for (int t = 0; t < 8; ++t) {
        const v4u x = g[t] ^ tg;
        bad |= (x[0] | x[1]) | (x[2] | x[3]);
        sum += __builtin_bit_cast(f32x4, x);
      }
      if (__all((bad & 255u) == 0) || spins > SPIN_CAP) break;
      asm volatile("" ::: "memory");
    }
    float rr = swap16_add(swap32_add(sum[0], sum[2]), swap32_add(sum[1], sum[3]));
    rr += dppf<XOR8>(rr);
    if ((lane & 8) == 0) __hip_atomic_store(optr, (unsigned short)(pack_bf16(rr, rr) & 0xffffu), RLX_AGENT);
    if (me == 0 && b == 0 && lane == 0) __hip_atomic_store((GAS unsigned*)((GAS char*)prm.ws + WS_EPOCH + blk * 256), tag, RLX_AGENT);
  }
}

extern "C" void kernel_launch(void* const* d_in, const int* in_sizes, int n_in, void* d_out, int out_size, void* d_ws,
                              size_t ws_size, hipStream_t stream) {
  Params p;
  p.x[0] = d_in[0];
  p.k[0] = d_in[1];
  p.v[0] = d_in[2];
  p.wq[0] = d_in[3];
  p.wo[0] = d_in[4];
  p.x[1] = d_in[5];
  p.k[1] = d_in[6];
  p.v[1] = d_in[7];
  p.wq[1] = d_in[8];
  p.wo[1] = d_in[9];
  p.out = d_out;
  p.ws = d_ws;
  mha2_decode_kernel<<<dim3(256), dim3(NTHR), 0, stream>>>(p);
}
```
